```python
import torch

def _softmax(x, axis=-1):
    return torch.softmax(x, dim=axis)
def _gelu(x):
    return torch.nn.functional.gelu(x, approximate="tanh")
def _silu(x):
    return torch.nn.functional.silu(x)
def _sigmoid(x):
    return torch.sigmoid(x)
def _layernorm(x, axis=-1, eps=1e-5):
    m = x.mean(dim=axis, keepdim=True)
    v = x.var(dim=axis, keepdim=True, unbiased=False)
    return (x - m) / torch.sqrt(v + eps)
def _scatter_add(tgt, idx, src, axis=0):
    return tgt.clone().index_add_(axis, idx, src)
def _segment_sum(src, seg, n):
    out = torch.zeros((n,) + src.shape[1:], dtype=src.dtype)
    return out.index_add_(0, seg, src)
def _pad(x, pw):
    flat = tuple(v for pair in reversed(pw) for v in pair)
    return torch.nn.functional.pad(x, flat)

kernel_name = 'interleave_layers__sliding_block_cached__dense_activation_gelu__decode'

def setup_inputs():

    _w = (lambda a: (lambda t: t.to(torch.bfloat16) if t.is_floating_point() else t)(torch.as_tensor(a)))
    def _A_setup():
        B, Sq, D = 2, 1, 576
        Hq, Hkv, Sk, Dh = 32, 16, 4096, 128
        assert Hq % Hkv == 0, (Hq, Hkv)
        return {
            "x": _w((lambda *s: torch.randn(*s, dtype=torch.bfloat16) * 0.05)(B, Sq, D)),
            "k_cache": _w((lambda *s: torch.randn(*s, dtype=torch.bfloat16) * 1.6)(B, Hkv, Sk, Dh)),
            "v_cache": _w((lambda *s: torch.randn(*s, dtype=torch.bfloat16) * 0.4)(B, Hkv, Sk, Dh)),
            "wq": _w((lambda *s: torch.randn(*s, dtype=torch.bfloat16) * 1.6)(D, Hq * Dh) * (D ** -0.5)),
            "wo": _w((lambda *s: torch.randn(*s, dtype=torch.bfloat16) * 0.4)(Hq * Dh, D) * ((Hq * Dh) ** -0.5)),
        }
    _A_in = _A_setup()

    def _B_setup():
        B, S, D, F = 2, 1, 576, 4608
        return {
            "x": _w((lambda *s: torch.randn(*s, dtype=torch.bfloat16) * 0.05)(B, S, D)),
            "w1": _w((lambda *s: torch.randn(*s, dtype=torch.bfloat16) * 0.4)(D, F) * (D ** -0.5)),
            "b1": _w((lambda *s: torch.randn(*s, dtype=torch.bfloat16) * 12.8)(F) * 0.01),
            "w2": _w((lambda *s: torch.randn(*s, dtype=torch.bfloat16) * 0.2)(F, D) * (F ** -0.5)),
            "b2": _w((lambda *s: torch.randn(*s, dtype=torch.bfloat16) * 0.8)(D) * 0.01),
        }
    _B_in = _B_setup()

    return {
        "A_x": _A_in["x"],
        "A_k_cache": _A_in["k_cache"],
        "A_v_cache": _A_in["v_cache"],
        "A_wq": _A_in["wq"],
        "A_wo": _A_in["wo"],
        "B_x": _B_in["x"],
        "B_w1": _B_in["w1"],
        "B_b1": _B_in["b1"],
        "B_w2": _B_in["w2"],
        "B_b2": _B_in["b2"],
    }

def reference(A_x, A_k_cache, A_v_cache, A_wq, A_wo, B_x, B_w1, B_b1, B_w2, B_b2):
    def _A_ref(x, k_cache, v_cache, wq, wo):
        B, Sq, D = x.shape
        Hq, Dh = 32, 128
        Hkv, Sk = k_cache.shape[1], k_cache.shape[2]
        W = min(4096, max(256, Sk // 4))
        rep = Hq // Hkv
        q = (lambda x, *d: x.permute(*d))((x @ wq).reshape(B, Sq, Hq, Dh), 0, 2, 1, 3)
        k = (lambda x, n, axis: torch.repeat_interleave(x, n, dim=axis))(k_cache[:, :, Sk - W:, :], rep, axis=1)
        v = (lambda x, n, axis: torch.repeat_interleave(x, n, dim=axis))(v_cache[:, :, Sk - W:, :], rep, axis=1)
        scores = torch.einsum("bhqd,bhkd->bhqk", q, k) * (Dh ** -0.5)
        p = _softmax(scores, axis=-1)
        o = torch.einsum("bhqk,bhkd->bhqd", p, v)
        return (lambda x, *d: x.permute(*d))(o, 0, 2, 1, 3).reshape(B, Sq, Hq * Dh) @ wo

    def _B_ref(x, w1, b1, w2, b2):
        h = torch.einsum("bsd,df->bsf", x, w1) + b1
        h = _gelu(h)
        return torch.einsum("bsf,fd->bsd", h, w2) + b2

    x = A_x
    x = x + _A_ref(_layernorm(x, axis=-1), A_k_cache, A_v_cache, A_wq, A_wo)
    x = x + _B_ref(_layernorm(x, axis=-1), B_w1, B_b1, B_w2, B_b2)
    x = x + _A_ref(_layernorm(x, axis=-1), A_k_cache, A_v_cache, A_wq, A_wo)
    x = x + _B_ref(_layernorm(x, axis=-1), B_w1, B_b1, B_w2, B_b2)
    return x
```

```cpp
#include <hip/hip_runtime.h>
#include <hip/hip_bf16.h>
#include <stdint.h>

typedef unsigned short u16;
__device__ __forceinline__ float bf2f(u16 b){ return __builtin_bit_cast(float, (unsigned)b << 16); }
__device__ __forceinline__ u16 f2bf(float f){ unsigned u = __builtin_bit_cast(unsigned, f); u += 0x7FFFu + ((u >> 16) & 1u); return (u16)(u >> 16); }
__device__ __forceinline__ float rbf(float f){ return bf2f(f2bf(f)); }

constexpr int D = 576, HQ = 32, HKV = 16, SK = 4096, DH = 128, W = 1024, F = 4608;

__device__ void ln_row(const float* x, float* xn, float* red) {
  int t = threadIdx.x;
  float s = 0.f;
  for (int i = t; i < D; i += blockDim.x) s += x[i];
  red[t] = s; __syncthreads();
  for (int o = blockDim.x / 2; o > 0; o >>= 1) { if (t < o) red[t] += red[t + o]; __syncthreads(); }
  float meanf = red[0] / D; __syncthreads();
  float m = rbf(meanf);
  s = 0.f;
  for (int i = t; i < D; i += blockDim.x) { float d = x[i] - meanf; s += d * d; }
  red[t] = s; __syncthreads();
  for (int o = blockDim.x / 2; o > 0; o >>= 1) { if (t < o) red[t] += red[t + o]; __syncthreads(); }
  float v = rbf(red[0] / D); __syncthreads();
  float den = rbf(sqrtf(rbf(v + 1e-5f)));
  for (int i = t; i < D; i += blockDim.x) xn[i] = rbf(rbf(x[i] - m) / den);
  __syncthreads();
}

__global__ void k_init(const u16* x, float* xf) { int i = blockIdx.x * 256 + threadIdx.x; if (i < 2 * D) xf[i] = bf2f(x[i]); }

__global__ void k_q(const float* xf, const u16* wq, float* q) {
  __shared__ float xn[D]; __shared__ float red[128];
  int h = blockIdx.x, b = blockIdx.y, t = threadIdx.x;
  ln_row(xf + b * D, xn, red);
  float acc = 0.f;
  for (int j = 0; j < D; ++j) acc += xn[j] * bf2f(wq[(size_t)j * (HQ * DH) + h * DH + t]);
  q[(b * HQ + h) * DH + t] = rbf(acc);
}
__global__ void k_att(const float* q, const u16* kc, const u16* vc, float* o) {
  __shared__ float qs[DH]; __shared__ float sc[W]; __shared__ float red[256];
  int h = blockIdx.x, b = blockIdx.y, t = threadIdx.x;
  int kvh = h / 2;
  if (t < DH) qs[t] = q[(b * HQ + h) * DH + t];
  __syncthreads();
  const u16* K = kc + ((size_t)(b * HKV + kvh) * SK + (SK - W)) * DH;
  const u16* V = vc + ((size_t)(b * HKV + kvh) * SK + (SK - W)) * DH;
  float mx = -1e30f;
  for (int k = t; k < W; k += 256) {
    float a = 0.f;
    for (int d = 0; d < DH; ++d) a += qs[d] * bf2f(K[(size_t)k * DH + d]);
    a = rbf(rbf(a) * 0.08838834764831845f);
    sc[k] = a; mx = fmaxf(mx, a);
  }
  red[t] = mx; __syncthreads();
  for (int s = 128; s > 0; s >>= 1) { if (t < s) red[t] = fmaxf(red[t], red[t + s]); __syncthreads(); }
  mx = red[0]; __syncthreads();
  float sum = 0.f;
  for (int k = t; k < W; k += 256) { float e = expf(sc[k] - mx); sc[k] = e; sum += e; }
  red[t] = sum; __syncthreads();
  for (int s = 128; s > 0; s >>= 1) { if (t < s) red[t] += red[t + s]; __syncthreads(); }
  sum = red[0]; __syncthreads();
  for (int k = t; k < W; k += 256) sc[k] = rbf(sc[k] / sum);
  __syncthreads();
  if (t < DH) {
    float a = 0.f;
    for (int k = 0; k < W; ++k) a += sc[k] * bf2f(V[(size_t)k * DH + t]);
    o[b * (HQ * DH) + h * DH + t] = rbf(a);
  }
}
__global__ void k_wo(const float* o, const u16* wo, float* xf) {
  __shared__ float red[256];
  int b = blockIdx.y, c = blockIdx.x * 64 + (threadIdx.x & 63), part = threadIdx.x >> 6;
  float a = 0.f;
  for (int k = part; k < HQ * DH; k += 4) a += o[b * HQ * DH + k] * bf2f(wo[(size_t)k * D + c]);
  red[threadIdx.x] = a; __syncthreads();
  if (part == 0) {
    a = red[threadIdx.x] + red[threadIdx.x + 64] + red[threadIdx.x + 128] + red[threadIdx.x + 192];
    xf[b * D + c] = rbf(xf[b * D + c] + rbf(a));
  }
}
__global__ void k_f1(const float* xf, const u16* w1, const u16* b1, float* hbuf) {
  __shared__ float xn[D]; __shared__ float red[256];
  int b = blockIdx.y, c = blockIdx.x * 256 + threadIdx.x;
  ln_row(xf + b * D, xn, red);
  float a = 0.f;
  for (int j = 0; j < D; ++j) a += xn[j] * bf2f(w1[(size_t)j * F + c]);
  a = rbf(rbf(a) + bf2f(b1[c]));
  float inner = 0.7978845608028654f * (a + 0.044715f * a * a * a);
  float g = 0.5f * a * (1.f + tanhf(inner));
  hbuf[b * F + c] = rbf(g);
}
__global__ void k_f2(const float* hbuf, const u16* w2, const u16* b2, float* xf, u16* out, int last) {
  __shared__ float red[256];
  int b = blockIdx.y, c = blockIdx.x * 64 + (threadIdx.x & 63), part = threadIdx.x >> 6;
  float a = 0.f;
  for (int k = part; k < F; k += 4) a += hbuf[b * F + k] * bf2f(w2[(size_t)k * D + c]);
  red[threadIdx.x] = a; __syncthreads();
  if (part == 0) {
    a = red[threadIdx.x] + red[threadIdx.x + 64] + red[threadIdx.x + 128] + red[threadIdx.x + 192];
    float y = rbf(rbf(a) + bf2f(b2[c]));
    float xo = rbf(xf[b * D + c] + y);
    xf[b * D + c] = xo;
    if (last) out[b * D + c] = f2bf(xo);
  }
}

extern "C" void kernel_launch(void* const* d_in, const int* in_sizes, int n_in,
                              void* d_out, int out_size, void* d_ws, size_t ws_size,
                              hipStream_t stream) {
  const u16* x = (const u16*)d_in[0];
  const u16* kc = (const u16*)d_in[1];
  const u16* vc = (const u16*)d_in[2];
  const u16* wq = (const u16*)d_in[3];
  const u16* wo = (const u16*)d_in[4];
  const u16* w1 = (const u16*)d_in[6];
  const u16* b1 = (const u16*)d_in[7];
  const u16* w2 = (const u16*)d_in[8];
  const u16* b2 = (const u16*)d_in[9];
  float* xf = (float*)d_ws;
  float* q = xf + 2048;
  float* o = q + 8192;
  float* hb = o + 8192;
  k_init<<<5, 256, 0, stream>>>(x, xf);
  for (int l = 0; l < 2; ++l) {
    k_q<<<dim3(HQ, 2), 128, 0, stream>>>(xf, wq, q);
    k_att<<<dim3(HQ, 2), 256, 0, stream>>>(q, kc, vc, o);
    k_wo<<<dim3(D / 64, 2), 256, 0, stream>>>(o, wo, xf);
    k_f1<<<dim3(F / 256, 2), 256, 0, stream>>>(xf, w1, b1, hb);
    k_f2<<<dim3(D / 64, 2), 256, 0, stream>>>(hb, w2, b2, xf, (u16*)d_out, l == 1);
  }
}
```

```cpp
#include <hip/hip_runtime.h>
#include <stdint.h>

typedef unsigned short u16;
typedef unsigned v4u __attribute__((ext_vector_type(4)));
typedef unsigned v2u __attribute__((ext_vector_type(2)));
typedef float f32x4 __attribute__((ext_vector_type(4)));
typedef __bf16 bf16x8 __attribute__((ext_vector_type(8)));
typedef __bf16 bf16x2 __attribute__((ext_vector_type(2)));
typedef short s16x4 __attribute__((ext_vector_type(4)));
#define LAS __attribute__((address_space(3)))
#define GAS __attribute__((address_space(1)))

#define STAMPS 0

namespace fk {

constexpr int D = 576, FF = 4608, SK = 4096, WIN = 1024, DH = 128;
constexpr int NR = 36;

constexpr unsigned WS_E = 0, WS_H0 = 4096;
constexpr unsigned Q_G = 8192, Q_L = 16 * Q_G;
constexpr unsigned Q0_OFF = WS_H0 + 256 * 128, Q0_G = 64 * 512;
constexpr unsigned WS_CB = Q0_OFF + 16 * Q0_G;
constexpr unsigned Q1_OFF = WS_CB;
constexpr unsigned O_OFF = Q1_OFF + Q_L, O_G = 16384, O_L = 16 * O_G;
constexpr unsigned ML_OFF = O_OFF + 2 * O_L, ML_G = 2048, ML_L = 16 * ML_G;
constexpr unsigned XP_OFF = ML_OFF + 2 * ML_L, XP_R = 64 * 128, XP_L = NR * XP_R;
constexpr unsigned X_REP = NR * 128;
constexpr unsigned X1_OFF = XP_OFF + 2 * XP_L, X1_L = 8 * X_REP;
constexpr unsigned XS1_OFF = X1_OFF + 2 * X1_L;
constexpr unsigned FP_OFF = XS1_OFF + 2 * X1_L, FP_R = 144 * 128, FP_L = NR * FP_R;
constexpr unsigned X2_OFF = FP_OFF + 2 * FP_L;
constexpr unsigned XS2_OFF = X2_OFF + 16 * X_REP;
constexpr unsigned CB_END = XS2_OFF + 16 * X_REP;
constexpr unsigned CLR_PER_CU = 12288;
static_assert(CB_END <= WS_CB + 256 * CLR_PER_CU, "clear region");
constexpr unsigned WS_USED = WS_CB + 256 * CLR_PER_CU;
constexpr unsigned WS_DBG = WS_USED;
constexpr unsigned OOB = 0x80000000u;

constexpr unsigned L_WQ = 0, L_V = 18432, L_WO = L_V + 32768, L_W1 = L_WO + 18432, L_W2 = L_W1 + 36864;
constexpr unsigned L_QI = L_W2 + 36864, L_OW = L_QI + 512, L_ML = L_OW + 4096, L_OI = L_ML + 64, L_RD = L_OI + 256;
constexpr unsigned L_RS = L_RD + 4 * 3 * 128, L_DUM = L_RS + 512, L_DBG = L_DUM + 2048, L_END = L_DBG + 16;
static_assert(L_END <= 163840, "LDS");

constexpr unsigned SENT = 0xFFFFFFFFu;
constexpr unsigned SPIN_CAP = 1u << 16;
constexpr int AUX_SC1 = 16, AUX_NT = 2;
constexpr unsigned ONES = 0x3F803F80u;

__device__ __forceinline__ float asf(unsigned u) { return __builtin_bit_cast(float, u); }
__device__ __forceinline__ unsigned asu(float f) { return __builtin_bit_cast(unsigned, f); }
__device__ __forceinline__ bf16x2 asbf2(unsigned u) { return __builtin_bit_cast(bf16x2, u); }
__device__ __forceinline__ unsigned pk(float lo, float hi) { bf16x2 v = {(__bf16)lo, (__bf16)hi}; return __builtin_bit_cast(unsigned, v); }
__device__ __forceinline__ float bflo(unsigned u) { return asf(u << 16); }
__device__ __forceinline__ float bfhi(unsigned u) { return asf(u & 0xFFFF0000u); }
__device__ __forceinline__ float rbf(float x) { return bflo(pk(x, x)); }
__device__ __forceinline__ unsigned san(unsigned u) { return u < 0xFFFFFFFEu ? u : 0xFFFFFFFEu; }
__device__ __forceinline__ f32x4 asf4(v4u v) { f32x4 o; o.x = asf(v.x); o.y = asf(v.y); o.z = asf(v.z); o.w = asf(v.w); return o; }
__device__ __forceinline__ v4u asu4(f32x4 v) { v4u o; o.x = asu(v.x); o.y = asu(v.y); o.z = asu(v.z); o.w = asu(v.w); return o; }
__device__ __forceinline__ unsigned umax3(unsigned a, unsigned b, unsigned c) { unsigned m = a > b ? a : b; return m > c ? m : c; }
__device__ __forceinline__ unsigned umax4(unsigned m, v4u v) { return umax3(umax3(m, v.x, v.y), v.z, v.w); }
__device__ __forceinline__ f32x4 mfma(v4u a, v4u b, f32x4 c) {
  return __builtin_amdgcn_mfma_f32_16x16x32_bf16(__builtin_bit_cast(bf16x8, a), __builtin_bit_cast(bf16x8, b), c, 0, 0, 0);
}
__device__ __forceinline__ float dot2(unsigned a, unsigned b, float c) { return __builtin_amdgcn_fdot2_f32_bf16(asbf2(a), asbf2(b), c, false); }
template <int CTRL> __device__ __forceinline__ unsigned dppu(unsigned x) { return (unsigned)__builtin_amdgcn_mov_dpp((int)x, CTRL, 0xf, 0xf, true); }
template <int CTRL> __device__ __forceinline__ float dpp(float x) { return asf(dppu<CTRL>(asu(x))); }
constexpr int XOR1 = 0xB1, XOR2 = 0x4E, ROR4 = 0x124, ROR8 = 0x128, SHL2 = 0x102, SHL4 = 0x104, SHL8 = 0x108, SHL12 = 0x10C;
__device__ __forceinline__ float t32_sum(float a, float b) { auto s = __builtin_amdgcn_permlane32_swap(asu(a), asu(b), false, false); unsigned x = s[0], y = s[1]; return asf(x) + asf(y); }
__device__ __forceinline__ float t16_sum(float a, float b) { auto s = __builtin_amdgcn_permlane16_swap(asu(a), asu(b), false, false); unsigned x = s[0], y = s[1]; return asf(x) + asf(y); }
__device__ __forceinline__ float trsum4(f32x4 v) { return t16_sum(t32_sum(v.x, v.y), t32_sum(v.z, v.w)); }
__device__ __forceinline__ float fmx(float a, float b) { return __builtin_elementwise_maximum(a, b); }
__device__ __forceinline__ float fmx3(float a, float b, float c) { return fmx(fmx(a, b), c); }
__device__ __forceinline__ float x16_max(float x) { auto s = __builtin_amdgcn_permlane16_swap(asu(x), asu(x), false, false); unsigned a = s[0], b = s[1]; return fmx(asf(a), asf(b)); }
__device__ __forceinline__ float x32_max(float x) { auto s = __builtin_amdgcn_permlane32_swap(asu(x), asu(x), false, false); unsigned a = s[0], b = s[1]; return fmx(asf(a), asf(b)); }
__device__ __forceinline__ unsigned pko(float lo, float hi) { unsigned u = pk(lo, hi); asm("" : "+v"(u)); return u; }
#define KEEP4(x) asm volatile("" :: "v"(x))
__device__ __forceinline__ float ex2(float x) { return __builtin_amdgcn_exp2f(x); }
__device__ __forceinline__ float rcp(float x) { return __builtin_amdgcn_rcpf(x); }
__device__ __forceinline__ float rdl(float x, int l) { return asf((unsigned)__builtin_amdgcn_readlane((int)asu(x), l)); }
constexpr float LOG2E = 1.4426950408889634f;

#define BAR() asm volatile("s_waitcnt lgkmcnt(0)\n\ts_barrier" ::: "memory")
#define VMWAIT(n) asm volatile("s_waitcnt vmcnt(" #n ")" ::: "memory")
#define CFENCE() asm volatile("" ::: "memory")
#define OPAQUE(x) asm volatile("" : "+v"(x))
#define PIN(x) asm volatile("" : "+v"(x))
#define SBAR() __builtin_amdgcn_sched_barrier(0)

__device__ __forceinline__ void glds(const void* base, unsigned voff, unsigned ldsd) {
  unsigned keep;
  asm volatile("s_mov_b32 %0, m0\n\ts_mov_b32 m0, %3\n\ts_nop 0\n\tglobal_load_lds_dwordx4 %1, %2 nt\n\ts_mov_b32 m0, %0"
               : "=&s"(keep) : "v"(voff), "s"(base), "s"(ldsd) : "memory");
}
__device__ __forceinline__ v4u trfrag(LAS unsigned char* L, unsigned a0, unsigned a1) {
  s16x4 x = __builtin_amdgcn_ds_read_tr16_b64_v4i16((LAS s16x4*)(L + a0));
  s16x4 y = __builtin_amdgcn_ds_read_tr16_b64_v4i16((LAS s16x4*)(L + a1));
  v2u ux = __builtin_bit_cast(v2u, x), uy = __builtin_bit_cast(v2u, y);
  v4u o; o.x = ux.x; o.y = ux.y; o.z = uy.x; o.w = uy.y; return o;
}
typedef __amdgpu_buffer_rsrc_t rsrc_t;
__device__ __forceinline__ v4u ld128(rsrc_t rs, unsigned v, unsigned s) { return __builtin_amdgcn_raw_buffer_load_b128(rs, (int)v, (int)s, AUX_SC1); }
__device__ __forceinline__ v2u ld64(rsrc_t rs, unsigned v, unsigned s) { return __builtin_amdgcn_raw_buffer_load_b64(rs, (int)v, (int)s, AUX_SC1); }
__device__ __forceinline__ void st128(rsrc_t rs, unsigned v, unsigned s, v4u d) { __builtin_amdgcn_raw_buffer_store_b128(d, rs, (int)v, (int)s, AUX_SC1); }
__device__ __forceinline__ void st64(rsrc_t rs, unsigned v, unsigned s, v2u d) { __builtin_amdgcn_raw_buffer_store_b64(d, rs, (int)v, (int)s, AUX_SC1); }
__device__ __forceinline__ void st32(rsrc_t rs, unsigned v, unsigned s, unsigned d) { __builtin_amdgcn_raw_buffer_store_b32(d, rs, (int)v, (int)s, AUX_SC1); }

struct Stats { float m, rden; };
__device__ __forceinline__ Stats mkstats(float sx, float sxx) {
  const float mean = sx * (1.0f / D);
  float var = sxx * (1.0f / D) - mean * mean;
  var = var > 0.f ? var : 0.f;
  const float den = rbf(__builtin_amdgcn_sqrtf(rbf(rbf(var) + 1e-5f)));
  Stats s; s.m = rbf(mean); s.rden = rcp(den); return s;
}
__device__ __forceinline__ float stats_reduce(v4u v) {
  float t = trsum4(asf4(v));
  t += dpp<ROR8>(t); t += dpp<ROR4>(t); t += dpp<XOR2>(t); t += dpp<XOR1>(t);
  return t;
}

#define STAMP(k) do {} while (0)
#define MIMIC 1
#define DLY_H1 6
#define DLY_H2 10
#define DLY_H3 12
#define DLY_H4 46
#define DLY_H5 40
#define DLY_H6R 14
#define DLY_H6F 42
#define DLY_H6I 90
#define PSLEEP 1
template <int N> __device__ __forceinline__ void delay() { if constexpr (N > 0) __builtin_amdgcn_s_sleep(N); }

#define POLL(LOADS, MAXV) do { LOADS; unsigned sp_ = 0; \
    while (__builtin_expect(__builtin_amdgcn_ballot_w64((MAXV) == SENT) != 0ull, 0)) { if (++sp_ > SPIN_CAP) break; __builtin_amdgcn_s_sleep(PSLEEP); CFENCE(); LOADS; } } while (0)

__global__ __launch_bounds__(256, 2) void fused_decode(const u16* xin, const u16* kc, const u16* vc, const u16* wq, const u16* wo,
                                                        const u16* w1, const u16* b1, const u16* w2, const u16* b2, u16* out,
                                                        unsigned char* ws) {
  extern __shared__ __attribute__((aligned(16))) unsigned char smem[];
  LAS unsigned char* L = (LAS unsigned char*)smem;
  const unsigned tid = threadIdx.x, lane = tid & 63, g = lane >> 4, r = lane & 15;
  const unsigned w = __builtin_amdgcn_readfirstlane(tid >> 6);
  const unsigned bid = blockIdx.x, xcd = bid & 7, slot = bid >> 3, c = xcd * 32 + slot;
  const unsigned kvh = c >> 4, ii = c & 15;
  STAMP(0);
  const unsigned cb = ii >> 2, kq = ii & 3;
  const unsigned ab = ii >> 3, asl = ii & 7;
  const unsigned rb = ii & 3, cb3 = ii >> 2;
  const unsigned fi = xcd * 18 + slot, ri = (slot - 18) * 8 + xcd;
  const bool isF = slot < 18, isR = slot >= 18 && ri < NR;
  const unsigned rep = xcd + ((isF || isR) ? 0u : 8u);
  const unsigned pc = lane & 7;
  const unsigned hu = ((r >> 1) & 4) * 4 + 4 * g + ((r >> 1) & 3);

  const rsrc_t rs = __builtin_amdgcn_make_buffer_rsrc((void*)ws, 0, (int)(WS_USED), 0x00020000);
  const v2u ep = ld64(rs, 0, WS_E);
  const rsrc_t rx = __builtin_amdgcn_make_buffer_rsrc((void*)xin, 0, 2 * D * 2, 0x00020000);
  v4u xf[5], xs[3];
  {
    const unsigned v = ((r & 1) * D + 144 * kq + 8 * g) * 2;
#pragma unroll
    for (int s = 0; s < 4; ++s) xf[s] = __builtin_amdgcn_raw_buffer_load_b128(rx, (int)(v + 64 * s), 0, 0);
    xf[4] = __builtin_amdgcn_raw_buffer_load_b128(rx, (int)(g < 2 ? v + 256 : OOB), 0, 0);
    const unsigned v2 = (((lane >> 1) & 1) * D + 16 * (lane >> 2) + 8 * (lane & 1)) * 2;
    xs[0] = __builtin_amdgcn_raw_buffer_load_b128(rx, (int)v2, 0, 0);
    xs[1] = __builtin_amdgcn_raw_buffer_load_b128(rx, (int)(v2 + 512), 0, 0);
    xs[2] = __builtin_amdgcn_raw_buffer_load_b128(rx, (int)(lane < 16 ? v2 + 1024 : OOB), 0, 0);
  }
  const rsrc_t rb2 = __builtin_amdgcn_make_buffer_rsrc((void*)b2, 0, D * 2, 0x00020000);
  const rsrc_t rb1 = __builtin_amdgcn_make_buffer_rsrc((void*)b1, 0, FF * 2, 0x00020000);
  const v2u xr0 = __builtin_amdgcn_raw_buffer_load_b64(rx, (int)(isR ? ((pc >> 2) * D + 16 * ri + 4 * (pc & 3)) * 2 : OOB), 0, 0);
  const v2u b2r = __builtin_amdgcn_raw_buffer_load_b64(rb2, (int)(isR ? (16 * ri + 4 * (pc & 3)) * 2 : OOB), 0, 0);
  const unsigned b1r = __builtin_amdgcn_raw_buffer_load_b16(rb1, (int)(isF ? (32 * fi + hu) * 2 : OOB), 0, 0);
  CFENCE();
  {
    const unsigned rowl = lane >> 3, s16 = lane & 7;
    const unsigned fsw = ((lane >> 4) & 1) | ((w & 1) << 1);
    const unsigned chunk = ((((s16 >> 1) ^ fsw) << 1) | (s16 & 1));
    const unsigned voff = (144 * kq + 8 * w + rowl) * 8192 + (kvh * 256 + 64 * cb) * 2 + chunk * 16;
#pragma unroll
    for (int k = 0; k < 4; ++k) glds(wq, voff + k * 32 * 8192, L_WQ + (w + 4 * k) * 1024);
    if (w < 2) glds(wq, voff + 4 * 32 * 8192, L_WQ + (w + 16) * 1024);
  }
  {
    const v4u ones = {SENT, SENT, SENT, SENT};
#pragma unroll
    for (int k = 0; k < 3; ++k) st128(rs, tid * 16 + k * 4096, WS_CB + c * CLR_PER_CU, ones);
  }
  v4u kf[8];
  {
    const u16* Kb = kc + ((size_t)(ab * 16 + kvh) * SK + (SK - WIN) + asl * 128 + w * 32) * DH;
    const rsrc_t rk = __builtin_amdgcn_make_buffer_rsrc((void*)Kb, 0, 32 * DH * 2, 0x00020000);
#pragma unroll
    for (int j = 0; j < 8; ++j) kf[j] = __builtin_amdgcn_raw_buffer_load_b128(rk, (int)(r * 256 + g * 16), (j >> 2) * 4096 + (j & 3) * 64, AUX_NT);
  }
  CFENCE();
  const unsigned q4 = r >> 2, p4 = r & 3;
  const unsigned jm = ((g & 1) << 1) | (g >> 1);
  unsigned vXf = (g >> 1) * 128 + (r & 1) * 32 + (g & 1) * 16;
  unsigned vXf4 = g < 2 ? vXf + 1024 : OOB;
  unsigned vXs = lane < NR ? lane * 128 : OOB;
  unsigned vRec = r < 2 ? r * 64 + g * 16 : OOB;
  unsigned vRed = (lane >> 3) * 128 + pc * 16;
  unsigned vRed4 = lane < 32 ? vRed + 4096 : OOB;
  unsigned vQ = (g * 16 + 4 * w + q4) * 128 + ab * 64 + p4 * 16;
  unsigned vO = (lane >> 3) * 1024 + pc * 16, vML = (lane >> 3) * 128;
  unsigned vXo = (lane >> 3) * X_REP + pc * 8;
  unsigned vSo = (pc & 3) == 0 ? (lane >> 3) * X_REP + (pc >> 2) * 8 : OOB;
  unsigned aWQ = L_WQ + (8 * g + q4) * 128 + ((w ^ (((q4 >> 1) & 1) | ((g & 1) << 1))) * 32) + 8 * p4;
  unsigned aWQ4 = g < 2 ? aWQ + 4 * 4096 : aWQ;
  unsigned aQIw = L_QI + (64 * w + 4 * r + jm) * 2, aQIr = L_QI + ((r & 1) * 128 + 8 * g) * 2;
  unsigned aV = L_V + w * 8192 + (4 * g + q4) * 256 + (4 * (g & 1) + q4) * 32 + 8 * p4;
  unsigned aOWw = g == 0 ? L_OW + (w * 128 + r) * 8 : L_DUM + lane * 8;
  unsigned aMw = (g == 0 && r < 2) ? L_ML + w * 16 + r * 4 : L_DUM + 1024 + lane * 4;
  unsigned aLw = lane == 0 ? L_ML + w * 16 + 8 : L_DUM + 1280 + lane * 8;
  unsigned aOWr = L_OW + (64 * (w & 1) + lane) * 8 + (w >> 1) * 4;
  unsigned aMr = L_ML + (w >> 1) * 4;
  unsigned aOIw = L_OI + ((w >> 1) * 64 + 32 * (w & 1) + 8 * (pc & 3) + 4 * (pc >> 2) + jm) * 2, aOIr = L_OI + ((r & 1) * 64 + 8 * g) * 2;
  unsigned aWO = L_WO + (4 * g + q4) * 288 + 32 * w + 8 * p4;
  unsigned aWO8 = L_WO + (4 * g + q4) * 288 + 32 * 8 + 8 * p4;
  unsigned aRSw = L_RS + (w * 32 + pc * 4 + jm) * 4, aRSr = L_RS + pc * 16;
  unsigned aW1 = L_W1 + w * 9216 + (8 * g + q4) * 64 + 8 * p4 + (g & 1) * 32;
  unsigned aW14 = g < 2 ? aW1 + 4 * 2048 : aW1;
  unsigned aW2 = L_W2 + w * 9216 + (4 * g + q4) * 288 + 8 * p4;
  unsigned aRDw = r < 3 ? L_RD + (w * 3 + r) * 128 + g * 16 : L_DUM + lane * 16;
  unsigned aRDr = L_RD + (r & 1) * 128 + hu * 4;
  PIN(vXf); PIN(vXf4); PIN(vXs); PIN(vRec); PIN(vRed); PIN(vRed4); PIN(vQ); PIN(vO); PIN(vML); PIN(vXo); PIN(vSo);
  PIN(aWQ); PIN(aWQ4); PIN(aQIw); PIN(aQIr); PIN(aV); PIN(aOWw); PIN(aMw); PIN(aLw); PIN(aOWr); PIN(aMr); PIN(aOIw); PIN(aOIr);
  PIN(aWO); PIN(aWO8); PIN(aRSw); PIN(aRSr); PIN(aW1); PIN(aW14); PIN(aW2); PIN(aRDw); PIN(aRDr);
  Stats st;
  {
    float sa = 0.f, sq = 0.f;
#pragma unroll
    for (int k = 0; k < 3; ++k) {
      sa = dot2(xs[k].x, ONES, sa); sa = dot2(xs[k].y, ONES, sa); sa = dot2(xs[k].z, ONES, sa); sa = dot2(xs[k].w, ONES, sa);
      sq = dot2(xs[k].x, xs[k].x, sq); sq = dot2(xs[k].y, xs[k].y, sq); sq = dot2(xs[k].z, xs[k].z, sq); sq = dot2(xs[k].w, xs[k].w, sq);
    }
    float t = t32_sum(sa, sq);
    { auto s = __builtin_amdgcn_permlane16_swap(asu(t), asu(t), false, false); unsigned a = s[0], b = s[1]; t = asf(a) + asf(b); }
    t += dpp<ROR8>(t); t += dpp<ROR4>(t); t += dpp<XOR1>(t);
    st = mkstats(rdl(t, ab * 2), rdl(t, 32 + ab * 2));
  }
  float xres[4], b2v[4];
  xres[0] = bflo(xr0.x); xres[1] = bfhi(xr0.x); xres[2] = bflo(xr0.y); xres[3] = bfhi(xr0.y);
  b2v[0] = bflo(b2r.x); b2v[1] = bfhi(b2r.x); b2v[2] = bflo(b2r.y); b2v[3] = bfhi(b2r.y);
  float b1v = bflo(b1r);
  PIN(st.m); PIN(st.rden); PIN(b1v); PIN(xres[0]); PIN(xres[1]); PIN(xres[2]); PIN(xres[3]); PIN(b2v[0]); PIN(b2v[1]); PIN(b2v[2]); PIN(b2v[3]);
  const unsigned tagl = ep.x + 1u, tagh = ep.y + (tagl == 0u ? 1u : 0u);
  STAMP(1);
  VMWAIT(8);
  BAR();
  if (tid == 0) st64(rs, c * 128, WS_H0, v2u{tagl, tagh});
  STAMP(2);
  v4u qv, vf[8];
  float cs = 0.f, cs1 = 0.f;

  {
    v4u wf[5];
#pragma unroll
    for (int s = 0; s < 4; ++s) wf[s] = trfrag(L, aWQ + s * 4096, aWQ + s * 4096 + 512);
    wf[4] = trfrag(L, aWQ4, aWQ4 + 512);
    if (r == 2) {
#pragma unroll
      for (int s = 0; s < 4; ++s) xf[s] = v4u{ONES, ONES, ONES, ONES};
      if (g < 2) xf[4] = v4u{ONES, ONES, ONES, ONES};
    }
    STAMP(3);
    f32x4 acc = {0.f, 0.f, 0.f, 0.f};
#pragma unroll
    for (int s = 0; s < 5; ++s) acc = mfma(wf[s], xf[s], acc);
    const unsigned vq0 = r < 3 ? r * 128 + g * 32 : OOB, sq0 = Q0_OFF + kvh * Q0_G + (kq * 16 + 4 * cb + w) * 512;
    st128(rs, vq0, sq0, v4u{asu(acc[0]), tagl, asu(acc[1]), tagl});
    st128(rs, vq0 + 16, sq0, v4u{asu(acc[2]), tagl, asu(acc[3]), tagl});
  }
  STAMP(4);
  {
    unsigned ln = lane; OPAQUE(ln);
    const u16* Vb = vc + ((size_t)(ab * 16 + kvh) * SK + (SK - WIN) + asl * 128 + w * 32) * DH;
    const unsigned gg = ln >> 4, rr = ln & 15;
    const unsigned v0 = gg * 256 + ((rr ^ (gg << 1)) * 16), v1 = (4 + gg) * 256 + ((rr ^ ((4 + gg) << 1)) * 16);
#pragma unroll
    for (int k = 0; k < 4; ++k) { glds(Vb, v0 + k * 2048, L_V + w * 8192 + k * 2048); glds(Vb, v1 + k * 2048, L_V + w * 8192 + k * 2048 + 1024); }
    STAMP(35);
    VMWAIT(3);
    const unsigned vq = (gg * 16 + 4 * w + ((ln >> 2) & 3)) * 512 + (ln & 3) * 32, sq = Q0_OFF + kvh * Q0_G;
    v4u qa, qb, ca, cb2;
    for (unsigned sp = 0;; ++sp) {
      CFENCE();
      const v2u t0 = ld64(rs, (w * 64 + ln) * 128, WS_H0);
      qa = ld128(rs, vq + ab * 128, sq); qb = ld128(rs, vq + ab * 128 + 16, sq);
      ca = ld128(rs, vq + 256, sq); cb2 = ld128(rs, vq + 272, sq);
      const bool ok = t0.x == tagl && t0.y == tagh && qa.y == tagl && qa.w == tagl && qb.y == tagl && qb.w == tagl &&
                      ca.y == tagl && ca.w == tagl && cb2.y == tagl && cb2.w == tagl;
      if (__all(ok) || sp > SPIN_CAP) break;
      __builtin_amdgcn_s_sleep(2);
    }
    STAMP(5);
    {
      const u16* Wb = wo + (size_t)(kvh * 256 + 64 * rb) * D + 144 * cb3;
#pragma unroll
      for (int k = 0; k < 5; ++k) {
        const unsigned piece = w + 4 * k;
        if (piece < 18) { const unsigned n = 64 * piece + ln, row = n / 18, ch = n - row * 18; glds(Wb, row * 1152 + ch * 16, L_WO + piece * 1024); }
      }
    }
    {
      unsigned a = aV; OPAQUE(a);
#pragma unroll
      for (int dt = 0; dt < 8; ++dt) vf[dt] = trfrag(L, a ^ (dt * 32), (a ^ (dt * 32)) + 16 * 256);
    }
    qv = v4u{qa.x, qa.z, qb.x, qb.z};
    cs = trsum4(f32x4{asf(ca.x), asf(ca.z), asf(cb2.x), asf(cb2.z)});
  }

#pragma unroll 1
  for (int lay = 0; lay < 2; ++lay) {
    {
      STAMP(6 + lay * 16);
      const float q = trsum4(asf4(qv));
      const unsigned qb = pk((q - st.m * cs) * st.rden, 0.f);
      *(LAS u16*)(L + aQIw) = (u16)qb;
    }
    BAR();
    {
      v4u qf[4];
#pragma unroll
      for (int s = 0; s < 4; ++s) qf[s] = *(LAS v4u*)(L + aQIr + 64 * s);
      f32x4 s0 = {0.f, 0.f, 0.f, 0.f}, s1 = {0.f, 0.f, 0.f, 0.f};
#pragma unroll
      for (int s = 0; s < 4; ++s) { s0 = mfma(kf[s], qf[s], s0); s1 = mfma(kf[4 + s], qf[s], s1); }
      float sc[8];
      {
        const float SC = 0.08838834764831845f;
        const unsigned u0 = pko(s0[0], s0[1]), u1 = pko(s0[2], s0[3]), u2 = pko(s1[0], s1[1]), u3 = pko(s1[2], s1[3]);
        const unsigned z0 = pko(bflo(u0) * SC, bfhi(u0) * SC), z1 = pko(bflo(u1) * SC, bfhi(u1) * SC);
        const unsigned z2 = pko(bflo(u2) * SC, bfhi(u2) * SC), z3 = pko(bflo(u3) * SC, bfhi(u3) * SC);
        sc[0] = bflo(z0); sc[1] = bfhi(z0); sc[2] = bflo(z1); sc[3] = bfhi(z1); sc[4] = bflo(z2); sc[5] = bfhi(z2); sc[6] = bflo(z3); sc[7] = bfhi(z3);
      }
      constexpr float EK = LOG2E;
      float mx = fmx3(fmx3(sc[0], sc[1], sc[2]), fmx3(sc[3], sc[4], sc[5]), fmx(sc[6], sc[7]));
      mx = x16_max(mx); mx = x32_max(mx);
      const float mb = -mx * EK;
      float e[8];
#pragma unroll
      for (int j = 0; j < 8; ++j) e[j] = ex2(__builtin_fmaf(sc[j], EK, mb));
      v4u pb; pb.x = pk(e[0], e[1]); pb.y = pk(e[2], e[3]); pb.z = pk(e[4], e[5]); pb.w = pk(e[6], e[7]);
      f32x4 oa[8];
#pragma unroll
      for (int dt = 0; dt < 8; ++dt) oa[dt] = mfma(pb, vf[dt], f32x4{0.f, 0.f, 0.f, 0.f});
      const f32x4 la = mfma(pb, v4u{ONES, ONES, ONES, ONES}, f32x4{0.f, 0.f, 0.f, 0.f});
#pragma unroll
      for (int dt = 0; dt < 8; ++dt) KEEP4(oa[dt]);
      KEEP4(la);
      *(LAS float*)(L + aMw) = mx * (EK / LOG2E);
#pragma unroll
      for (int dt = 0; dt < 8; ++dt) { v2u t; t.x = asu(oa[dt][0]); t.y = asu(oa[dt][1]); *(LAS v2u*)(L + aOWw + dt * 128) = t; }
      { v2u t; t.x = asu(la[0]); t.y = asu(la[1]); *(LAS v2u*)(L + aLw) = t; }
    }
    BAR();
    {
      const unsigned head = w >> 1;
      float m[4], l[4], o[4];
#pragma unroll
      for (int k = 0; k < 4; ++k) {
        m[k] = *(LAS float*)(L + aMr + k * 16);
        l[k] = *(LAS float*)(L + aMr + k * 16 + 8);
        o[k] = *(LAS float*)(L + aOWr + k * 1024);
      }
      const float M = fmx3(fmx(m[0], m[1]), m[2], m[3]);
      const float mb = -M * LOG2E;
      float f[4];
#pragma unroll
      for (int k = 0; k < 4; ++k) f[k] = ex2(__builtin_fmaf(m[k], LOG2E, mb));
      const float os = __builtin_fmaf(f[0], o[0], f[1] * o[1]) + __builtin_fmaf(f[2], o[2], f[3] * o[3]);
      const float lsum = __builtin_fmaf(f[0], l[0], f[1] * l[1]) + __builtin_fmaf(f[2], l[2], f[3] * l[3]);
      const unsigned sidx = (ab * 8 + asl) * 2 + head;
      st32(rs, lane * 4, O_OFF + lay * O_L + kvh * O_G + sidx * 512 + (w & 1) * 256, asu(os));
      v2u ml; ml.x = asu(M); ml.y = asu(lsum);
      st64(rs, ((w & 1) == 0 && lane == 0) ? 0u : OOB, ML_OFF + lay * ML_L + kvh * ML_G + sidx * 64, ml);
    }
    STAMP(7 + lay * 16);
    {
      v4u wof[6];
      const unsigned head = rb >> 1, b = w >> 1, sub = w & 1;
      v4u ov; v2u ml;
      delay<DLY_H2>();
      const unsigned soO = O_OFF + lay * O_L + kvh * O_G + (b * 16 + head) * 512 + (rb & 1) * 256 + sub * 128;
      const unsigned soM = ML_OFF + lay * ML_L + kvh * ML_G + (b * 16 + head) * 64;
      POLL(ov = ld128(rs, vO, soO); ml = ld64(rs, vML, soM), umax3(umax4(0u, ov), ml.x, ml.y));
      STAMP(8 + lay * 16);
      const float ms = asf(ml.x);
      float M = fmx(ms, dpp<ROR8>(ms)); M = x16_max(M); M = x32_max(M);
      const float f = ex2((ms - M) * LOG2E);
      float lsum = f * asf(ml.y);
      f32x4 o = asf4(ov) * f;
      lsum += dpp<ROR8>(lsum);
      float t = trsum4(o);
      { auto s = __builtin_amdgcn_permlane16_swap(asu(lsum), asu(lsum), false, false); unsigned x = s[0], y = s[1]; lsum = asf(x) + asf(y); }
      t += dpp<ROR8>(t);
      { auto s = __builtin_amdgcn_permlane32_swap(asu(lsum), asu(lsum), false, false); unsigned x = s[0], y = s[1]; lsum = asf(x) + asf(y); }
      const unsigned ob = pk(t * rcp(lsum), 0.f);
      *(LAS u16*)(L + aOIw) = (u16)ob;
      if (lay == 0) VMWAIT(0);
      BAR();
      const v4u of0 = *(LAS v4u*)(L + aOIr), of1 = *(LAS v4u*)(L + aOIr + 64);
      wof[0] = trfrag(L, aWO, aWO + 16 * 288); wof[2] = trfrag(L, aWO + 128, aWO + 128 + 16 * 288); wof[4] = trfrag(L, aWO8, aWO8 + 16 * 288);
      wof[1] = trfrag(L, aWO + 32 * 288, aWO + 48 * 288); wof[3] = trfrag(L, aWO + 128 + 32 * 288, aWO + 128 + 48 * 288);
      wof[5] = trfrag(L, aWO8 + 32 * 288, aWO8 + 48 * 288);
      const unsigned so = XP_OFF + lay * XP_L + 9 * cb3 * XP_R + (kvh * 4 + rb) * 128;
      f32x4 acc[3];
#pragma unroll
      for (int k = 0; k < 3; ++k) acc[k] = mfma(wof[2 * k], of0, f32x4{0.f, 0.f, 0.f, 0.f});
#pragma unroll
      for (int k = 0; k < 3; ++k) acc[k] = mfma(wof[2 * k + 1], of1, acc[k]);
      st128(rs, vRec, so + w * XP_R, asu4(acc[0]));
      st128(rs, vRec, so + (w + 4) * XP_R, asu4(acc[1]));
      if (w == 0) st128(rs, vRec, so + 8 * XP_R, asu4(acc[2]));
    }
    STAMP(9 + lay * 16);
    if (lay == 0 && isF) {
      unsigned ln = lane; OPAQUE(ln);
      const unsigned s4 = ln & 3, chunk = (((s4 >> 1) ^ ((ln >> 5) & 1)) << 1) | (s4 & 1);
      const unsigned v1 = (144 * w + (ln >> 2)) * (FF * 2) + 64 * fi + chunk * 16;
#pragma unroll
      for (int k = 0; k < 9; ++k) glds(w1, v1 + k * 16 * (FF * 2), L_W1 + w * 9216 + k * 1024);
      const u16* W2b = w2 + (size_t)(32 * fi) * D + 144 * w;
#pragma unroll
      for (int k = 0; k < 9; ++k) { const unsigned n = 64 * k + ln, row = n / 18, ch = n - row * 18; glds(W2b, row * 1152 + ch * 16, L_W2 + w * 9216 + k * 1024); }
    }
    if (isR) {
      v4u pv[2];
      delay<DLY_H3>();
      const unsigned so = XP_OFF + lay * XP_L + ri * XP_R + w * 2048;
      POLL(pv[0] = ld128(rs, vRed, so); pv[1] = ld128(rs, vRed + 1024, so), umax4(umax4(0u, pv[0]), pv[1]));
      STAMP(10 + lay * 16);
      float t = trsum4(asf4(pv[0]) + asf4(pv[1]));
      t += dpp<ROR8>(t);
      *(LAS float*)(L + aRSw) = t;
      BAR();
      if (w == 0) {
        const f32x4 s0 = *(LAS f32x4*)(L + aRSr), s1 = *(LAS f32x4*)(L + aRSr + 128), s2 = *(LAS f32x4*)(L + aRSr + 256), s3 = *(LAS f32x4*)(L + aRSr + 384);
        const f32x4 s = (s0 + s1) + (s2 + s3);
        const unsigned y0 = pk(s[0], s[1]), y1 = pk(s[2], s[3]);
        v2u xo; xo.x = san(pk(xres[0] + bflo(y0), xres[1] + bfhi(y0))); xo.y = san(pk(xres[2] + bflo(y1), xres[3] + bfhi(y1)));
        st64(rs, vXo, X1_OFF + lay * X1_L + ri * 128, xo);
        xres[0] = bflo(xo.x); xres[1] = bfhi(xo.x); xres[2] = bflo(xo.y); xres[3] = bfhi(xo.y);
        float sa = (xres[0] + xres[1]) + (xres[2] + xres[3]);
        float sq = __builtin_fmaf(xres[0], xres[0], xres[1] * xres[1]) + __builtin_fmaf(xres[2], xres[2], xres[3] * xres[3]);
        sa += dpp<XOR1>(sa); sq += dpp<XOR1>(sq); sa += dpp<XOR2>(sa); sq += dpp<XOR2>(sq);
        v2u so2; so2.x = asu(sa); so2.y = asu(sq);
        st64(rs, vSo, XS1_OFF + lay * X1_L + ri * 128, so2);
      }
      STAMP(11 + lay * 16);
    }
    if (isF) {
      v4u a0[5], a1[5];
      delay<DLY_H4>();
      if (lay == 0) VMWAIT(0);
#pragma unroll
      for (int s = 0; s < 4; ++s) {
        a0[s] = trfrag(L, aW1 + s * 2048, aW1 + s * 2048 + 256);
        a1[s] = trfrag(L, (aW1 ^ 32) + s * 2048, (aW1 ^ 32) + s * 2048 + 256);
      }
      a0[4] = trfrag(L, aW14, aW14 + 256); a1[4] = trfrag(L, aW14 ^ 32, (aW14 ^ 32) + 256);
      v4u x1[5], s1v;
      const unsigned soS = XS1_OFF + lay * X1_L + rep * X_REP;
      {
        const unsigned so = X1_OFF + lay * X1_L + rep * X_REP + 9 * w * 128;
        POLL(
          for (int s = 0; s < 4; ++s) x1[s] = ld128(rs, vXf + s * 256, so);
          x1[4] = ld128(rs, vXf4, so); s1v = ld128(rs, vXs, soS),
          umax4(umax4(umax4(umax4(umax4(umax4(0u, x1[0]), x1[1]), x1[2]), x1[3]), x1[4]), s1v));
      }
      if (lay == 0 && r == 2) {
#pragma unroll
        for (int s = 0; s < 4; ++s) x1[s] = v4u{ONES, ONES, ONES, ONES};
        if (g < 2) x1[4] = v4u{ONES, ONES, ONES, ONES};
      }
      STAMP(12 + lay * 16);
      f32x4 h0 = {0.f, 0.f, 0.f, 0.f}, h1 = {0.f, 0.f, 0.f, 0.f};
#pragma unroll
      for (int s = 0; s < 5; ++s) { h0 = mfma(a0[s], x1[s], h0); h1 = mfma(a1[s], x1[s], h1); }
      v4u w2f[9];
#pragma unroll
      for (int t = 0; t < 9; ++t) w2f[t] = trfrag(L, aW2 + 32 * t, aW2 + 32 * t + 16 * 288);
      float sm, sr;
      {
        const float t = stats_reduce(s1v);
        auto s = __builtin_amdgcn_permlane32_swap(asu(t), asu(t), false, false);
        const unsigned x = s[0], y = s[1];
        const Stats z = mkstats(asf(x), asf(y));
        const float m0 = rdl(z.m, 0), m1 = rdl(z.m, 16), r0 = rdl(z.rden, 0), r1 = rdl(z.rden, 16);
        sm = (r & 1) ? m1 : m0; sr = (r & 1) ? r1 : r0;
      }
      *(LAS f32x4*)(L + aRDw) = h0;
      *(LAS f32x4*)(L + aRDw + 64) = h1;
      BAR();
      const float hs = (*(LAS float*)(L + aRDr) + *(LAS float*)(L + aRDr + 384)) + (*(LAS float*)(L + aRDr + 768) + *(LAS float*)(L + aRDr + 1152));
      if (lay == 0) {
        const unsigned ac = L_RD + 2 * 128 + hu * 4;
        cs1 = (*(LAS float*)(L + ac) + *(LAS float*)(L + ac + 384)) + (*(LAS float*)(L + ac + 768) + *(LAS float*)(L + ac + 1152));
      }
      const float pre = rbf((hs - sm * cs1) * sr);
      const float a = rbf(pre + b1v);
      const float u = a * __builtin_fmaf(a * a, 0.7978845608028654f * 0.044715f, 0.7978845608028654f);
      const float hv = a * rcp(1.0f + ex2(-2.0f * LOG2E * u));
      const unsigned hp = pk(hv, dpp<SHL2>(hv));
      v4u hb; hb.x = hp; hb.y = dppu<SHL4>(hp); hb.z = dppu<SHL8>(hp); hb.w = dppu<SHL12>(hp);
      const unsigned so = FP_OFF + lay * FP_L + 9 * w * FP_R + fi * 128;
      f32x4 acc[9];
#pragma unroll
      for (int t = 0; t < 9; ++t) acc[t] = mfma(w2f[t], hb, f32x4{0.f, 0.f, 0.f, 0.f});
      SBAR();
#pragma unroll
      for (int t = 0; t < 9; ++t) st128(rs, vRec, so + t * FP_R, asu4(acc[t]));
      STAMP(13 + lay * 16);
    }
    if (isR) {
      v4u pv[5];
      delay<DLY_H5>();
      const unsigned so = FP_OFF + lay * FP_L + ri * FP_R + w * 36 * 128;
      POLL(
        for (int k = 0; k < 4; ++k) pv[k] = ld128(rs, vRed + k * 1024, so);
        pv[4] = ld128(rs, vRed4, so),
        umax4(umax4(umax4(umax4(umax4(0u, pv[0]), pv[1]), pv[2]), pv[3]), pv[4]));
      STAMP(14 + lay * 16);
      float t = trsum4(((asf4(pv[0]) + asf4(pv[1])) + (asf4(pv[2]) + asf4(pv[3]))) + asf4(pv[4]));
      t += dpp<ROR8>(t);
      *(LAS float*)(L + aRSw) = t;
      BAR();
      if (w == 0) {
        const f32x4 s0 = *(LAS f32x4*)(L + aRSr), s1 = *(LAS f32x4*)(L + aRSr + 128), s2 = *(LAS f32x4*)(L + aRSr + 256), s3 = *(LAS f32x4*)(L + aRSr + 384);
        const f32x4 s = (s0 + s1) + (s2 + s3);
        const unsigned y0 = pk(s[0], s[1]), y1 = pk(s[2], s[3]);
        const unsigned z0 = pk(bflo(y0) + b2v[0], bfhi(y0) + b2v[1]), z1 = pk(bflo(y1) + b2v[2], bfhi(y1) + b2v[3]);
        v2u xo; xo.x = pk(xres[0] + bflo(z0), xres[1] + bfhi(z0)); xo.y = pk(xres[2] + bflo(z1), xres[3] + bfhi(z1));
        if (lay == 0) {
          xo.x = san(xo.x); xo.y = san(xo.y);
          st64(rs, vXo, X2_OFF + ri * 128, xo);
          st64(rs, vXo, X2_OFF + 8 * X_REP + ri * 128, xo);
          xres[0] = bflo(xo.x); xres[1] = bfhi(xo.x); xres[2] = bflo(xo.y); xres[3] = bfhi(xo.y);
          float sa = (xres[0] + xres[1]) + (xres[2] + xres[3]);
          float sq = __builtin_fmaf(xres[0], xres[0], xres[1] * xres[1]) + __builtin_fmaf(xres[2], xres[2], xres[3] * xres[3]);
          sa += dpp<XOR1>(sa); sq += dpp<XOR1>(sq); sa += dpp<XOR2>(sa); sq += dpp<XOR2>(sq);
          v2u so2; so2.x = asu(sa); so2.y = asu(sq);
          st64(rs, vSo, XS2_OFF + ri * 128, so2);
          st64(rs, vSo, XS2_OFF + 8 * X_REP + ri * 128, so2);
        } else if (lane < 8) {
          *(GAS v2u*)(out + (pc >> 2) * D + 16 * ri + 4 * (pc & 3)) = xo;
        }
      }
      STAMP(15 + lay * 16);
    }
    if (lay == 0) {
      v4u wf[5], xg[5], sv;
#pragma unroll
      for (int s = 0; s < 4; ++s) wf[s] = trfrag(L, aWQ + s * 4096, aWQ + s * 4096 + 512);
      wf[4] = trfrag(L, aWQ4, aWQ4 + 512);
      if (isR) delay<DLY_H6R>(); else if (isF) delay<DLY_H6F>(); else delay<DLY_H6I>();
      const unsigned so = X2_OFF + rep * X_REP + 9 * kq * 128;
      POLL(
        for (int s = 0; s < 4; ++s) xg[s] = ld128(rs, vXf + s * 256, so);
        xg[4] = ld128(rs, vXf4, so); sv = ld128(rs, vXs, XS2_OFF + rep * X_REP),
        umax4(umax4(umax4(umax4(umax4(umax4(0u, xg[0]), xg[1]), xg[2]), xg[3]), xg[4]), sv));
      STAMP(19);
      f32x4 acc = {0.f, 0.f, 0.f, 0.f};
#pragma unroll
      for (int s = 0; s < 5; ++s) acc = mfma(wf[s], xg[s], acc);
      st128(rs, vRec, Q1_OFF + kvh * Q_G + (kq * 16 + 4 * cb + w) * 128, asu4(acc));
      STAMP(20);
      {
        unsigned a = aV; OPAQUE(a);
#pragma unroll
        for (int dt = 0; dt < 8; ++dt) vf[dt] = trfrag(L, a ^ (dt * 32), (a ^ (dt * 32)) + 16 * 256);
      }
      const float t = stats_reduce(sv);
      st = mkstats(rdl(t, 16 * ab), rdl(t, 32 + 16 * ab));
      STAMP(21);
      delay<DLY_H1>();
      POLL(qv = ld128(rs, vQ, Q1_OFF + kvh * Q_G), umax4(0u, qv));
    }
  }
  if (c == 0 && tid == 0) st64(rs, 0, WS_E, v2u{tagl, tagh});
}

}

extern "C" void kernel_launch(void* const* d_in, const int* in_sizes, int n_in, void* d_out, int out_size, void* d_ws, size_t ws_size,
                              hipStream_t stream) {
  (void)in_sizes; (void)n_in; (void)out_size; (void)ws_size;
  (void)hipFuncSetAttribute((const void*)fk::fused_decode, hipFuncAttributeMaxDynamicSharedMemorySize, (int)fk::L_END);
  fk::fused_decode<<<256, 256, fk::L_END, stream>>>((const u16*)d_in[0], (const u16*)d_in[1], (const u16*)d_in[2], (const u16*)d_in[3],
                                            (const u16*)d_in[4], (const u16*)d_in[6], (const u16*)d_in[7], (const u16*)d_in[8],
                                            (const u16*)d_in[9], (u16*)d_out, (unsigned char*)d_ws);
}
```

```cpp
#include <hip/hip_runtime.h>
#include <stdint.h>
#include <stdio.h>
#define STAMPS 1

typedef unsigned short u16;
typedef unsigned v4u __attribute__((ext_vector_type(4)));
typedef unsigned v2u __attribute__((ext_vector_type(2)));
typedef float f32x4 __attribute__((ext_vector_type(4)));
typedef __bf16 bf16x8 __attribute__((ext_vector_type(8)));
typedef __bf16 bf16x2 __attribute__((ext_vector_type(2)));
typedef short s16x4 __attribute__((ext_vector_type(4)));
#define LAS __attribute__((address_space(3)))
#define GAS __attribute__((address_space(1)))

namespace fk {

constexpr int D = 576, FF = 4608, SK = 4096, WIN = 1024, DH = 128;
constexpr int NR = 36;

constexpr unsigned WS_E = 0, WS_H0 = 4096;
constexpr unsigned Q_G = 8192, Q_L = 16 * Q_G;
constexpr unsigned Q0_OFF = WS_H0 + 256 * 128, Q0_G = 64 * 512;
constexpr unsigned WS_CB = Q0_OFF + 16 * Q0_G;
constexpr unsigned Q1_OFF = WS_CB;
constexpr unsigned O_OFF = Q1_OFF + Q_L, O_G = 16384, O_L = 16 * O_G;
constexpr unsigned ML_OFF = O_OFF + 2 * O_L, ML_G = 2048, ML_L = 16 * ML_G;
constexpr unsigned XP_OFF = ML_OFF + 2 * ML_L, XP_R = 64 * 128, XP_L = NR * XP_R;
constexpr unsigned X_REP = NR * 128;
constexpr unsigned X1_OFF = XP_OFF + 2 * XP_L, X1_L = 8 * X_REP;
constexpr unsigned XS1_OFF = X1_OFF + 2 * X1_L;
constexpr unsigned FP_OFF = XS1_OFF + 2 * X1_L, FP_R = 144 * 128, FP_L = NR * FP_R;
constexpr unsigned X2_OFF = FP_OFF + 2 * FP_L;
constexpr unsigned XS2_OFF = X2_OFF + 16 * X_REP;
constexpr unsigned CB_END = XS2_OFF + 16 * X_REP;
constexpr unsigned CLR_PER_CU = 12288;
static_assert(CB_END <= WS_CB + 256 * CLR_PER_CU, "clear region");
constexpr unsigned WS_USED = WS_CB + 256 * CLR_PER_CU;
constexpr unsigned WS_DBG = WS_USED;
constexpr unsigned OOB = 0x80000000u;

constexpr unsigned L_WQ = 0, L_V = 18432, L_WO = L_V + 32768, L_W1 = L_WO + 18432, L_W2 = L_W1 + 36864;
constexpr unsigned L_QI = L_W2 + 36864, L_OW = L_QI + 512, L_ML = L_OW + 4096, L_OI = L_ML + 64, L_RD = L_OI + 256;
constexpr unsigned L_RS = L_RD + 4 * 3 * 128, L_DUM = L_RS + 512, L_DBG = L_DUM + 2048, L_END = L_DBG + 16;
static_assert(L_END <= 163840, "LDS");

constexpr unsigned SENT = 0xFFFFFFFFu;
constexpr unsigned SPIN_CAP = 1u << 16;
constexpr int AUX_SC1 = 16, AUX_NT = 2;
constexpr unsigned ONES = 0x3F803F80u;

__device__ __forceinline__ float asf(unsigned u) { return __builtin_bit_cast(float, u); }
__device__ __forceinline__ unsigned asu(float f) { return __builtin_bit_cast(unsigned, f); }
__device__ __forceinline__ bf16x2 asbf2(unsigned u) { return __builtin_bit_cast(bf16x2, u); }
__device__ __forceinline__ unsigned pk(float lo, float hi) { bf16x2 v = {(__bf16)lo, (__bf16)hi}; return __builtin_bit_cast(unsigned, v); }
__device__ __forceinline__ float bflo(unsigned u) { return asf(u << 16); }
__device__ __forceinline__ float bfhi(unsigned u) { return asf(u & 0xFFFF0000u); }
__device__ __forceinline__ float rbf(float x) { return bflo(pk(x, x)); }
__device__ __forceinline__ unsigned san(unsigned u) { return u < 0xFFFFFFFEu ? u : 0xFFFFFFFEu; }
__device__ __forceinline__ f32x4 asf4(v4u v) { f32x4 o; o.x = asf(v.x); o.y = asf(v.y); o.z = asf(v.z); o.w = asf(v.w); return o; }
__device__ __forceinline__ v4u asu4(f32x4 v) { v4u o; o.x = asu(v.x); o.y = asu(v.y); o.z = asu(v.z); o.w = asu(v.w); return o; }
__device__ __forceinline__ unsigned umax3(unsigned a, unsigned b, unsigned c) { unsigned m = a > b ? a : b; return m > c ? m : c; }
__device__ __forceinline__ unsigned umax4(unsigned m, v4u v) { return umax3(umax3(m, v.x, v.y), v.z, v.w); }
__device__ __forceinline__ f32x4 mfma(v4u a, v4u b, f32x4 c) {
  return __builtin_amdgcn_mfma_f32_16x16x32_bf16(__builtin_bit_cast(bf16x8, a), __builtin_bit_cast(bf16x8, b), c, 0, 0, 0);
}
__device__ __forceinline__ float dot2(unsigned a, unsigned b, float c) { return __builtin_amdgcn_fdot2_f32_bf16(asbf2(a), asbf2(b), c, false); }
template <int CTRL> __device__ __forceinline__ unsigned dppu(unsigned x) { return (unsigned)__builtin_amdgcn_mov_dpp((int)x, CTRL, 0xf, 0xf, true); }
template <int CTRL> __device__ __forceinline__ float dpp(float x) { return asf(dppu<CTRL>(asu(x))); }
constexpr int XOR1 = 0xB1, XOR2 = 0x4E, ROR4 = 0x124, ROR8 = 0x128, SHL2 = 0x102, SHL4 = 0x104, SHL8 = 0x108, SHL12 = 0x10C;
__device__ __forceinline__ float t32_sum(float a, float b) { auto s = __builtin_amdgcn_permlane32_swap(asu(a), asu(b), false, false); unsigned x = s[0], y = s[1]; return asf(x) + asf(y); }
__device__ __forceinline__ float t16_sum(float a, float b) { auto s = __builtin_amdgcn_permlane16_swap(asu(a), asu(b), false, false); unsigned x = s[0], y = s[1]; return asf(x) + asf(y); }
__device__ __forceinline__ float trsum4(f32x4 v) { return t16_sum(t32_sum(v.x, v.y), t32_sum(v.z, v.w)); }
__device__ __forceinline__ float fmx(float a, float b) { return __builtin_elementwise_maximum(a, b); }
__device__ __forceinline__ float fmx3(float a, float b, float c) { return fmx(fmx(a, b), c); }
__device__ __forceinline__ float x16_max(float x) { auto s = __builtin_amdgcn_permlane16_swap(asu(x), asu(x), false, false); unsigned a = s[0], b = s[1]; return fmx(asf(a), asf(b)); }
__device__ __forceinline__ float x32_max(float x) { auto s = __builtin_amdgcn_permlane32_swap(asu(x), asu(x), false, false); unsigned a = s[0], b = s[1]; return fmx(asf(a), asf(b)); }
__device__ __forceinline__ unsigned pko(float lo, float hi) { unsigned u = pk(lo, hi); asm("" : "+v"(u)); return u; }
#define KEEP4(x) asm volatile("" :: "v"(x))
__device__ __forceinline__ float ex2(float x) { return __builtin_amdgcn_exp2f(x); }
__device__ __forceinline__ float rcp(float x) { return __builtin_amdgcn_rcpf(x); }
__device__ __forceinline__ float rdl(float x, int l) { return asf((unsigned)__builtin_amdgcn_readlane((int)asu(x), l)); }
constexpr float LOG2E = 1.4426950408889634f;

#define BAR() asm volatile("s_waitcnt lgkmcnt(0)\n\ts_barrier" ::: "memory")
#define VMWAIT(n) asm volatile("s_waitcnt vmcnt(" #n ")" ::: "memory")
#define CFENCE() asm volatile("" ::: "memory")
#define OPAQUE(x) asm volatile("" : "+v"(x))
#define PIN(x) asm volatile("" : "+v"(x))
#define SBAR() __builtin_amdgcn_sched_barrier(0)

__device__ __forceinline__ void glds(const void* base, unsigned voff, unsigned ldsd) {
  unsigned keep;
  asm volatile("s_mov_b32 %0, m0\n\ts_mov_b32 m0, %3\n\ts_nop 0\n\tglobal_load_lds_dwordx4 %1, %2 nt\n\ts_mov_b32 m0, %0"
               : "=&s"(keep) : "v"(voff), "s"(base), "s"(ldsd) : "memory");
}
__device__ __forceinline__ v4u trfrag(LAS unsigned char* L, unsigned a0, unsigned a1) {
  s16x4 x = __builtin_amdgcn_ds_read_tr16_b64_v4i16((LAS s16x4*)(L + a0));
  s16x4 y = __builtin_amdgcn_ds_read_tr16_b64_v4i16((LAS s16x4*)(L + a1));
  v2u ux = __builtin_bit_cast(v2u, x), uy = __builtin_bit_cast(v2u, y);
  v4u o; o.x = ux.x; o.y = ux.y; o.z = uy.x; o.w = uy.y; return o;
}
typedef __amdgpu_buffer_rsrc_t rsrc_t;
__device__ __forceinline__ v4u ld128(rsrc_t rs, unsigned v, unsigned s) { return __builtin_amdgcn_raw_buffer_load_b128(rs, (int)v, (int)s, AUX_SC1); }
__device__ __forceinline__ v2u ld64(rsrc_t rs, unsigned v, unsigned s) { return __builtin_amdgcn_raw_buffer_load_b64(rs, (int)v, (int)s, AUX_SC1); }
__device__ __forceinline__ void st128(rsrc_t rs, unsigned v, unsigned s, v4u d) { __builtin_amdgcn_raw_buffer_store_b128(d, rs, (int)v, (int)s, AUX_SC1); }
__device__ __forceinline__ void st64(rsrc_t rs, unsigned v, unsigned s, v2u d) { __builtin_amdgcn_raw_buffer_store_b64(d, rs, (int)v, (int)s, AUX_SC1); }
__device__ __forceinline__ void st32(rsrc_t rs, unsigned v, unsigned s, unsigned d) { __builtin_amdgcn_raw_buffer_store_b32(d, rs, (int)v, (int)s, AUX_SC1); }

struct Stats { float m, rden; };
__device__ __forceinline__ Stats mkstats(float sx, float sxx) {
  const float mean = sx * (1.0f / D);
  float var = sxx * (1.0f / D) - mean * mean;
  var = var > 0.f ? var : 0.f;
  const float den = rbf(__builtin_amdgcn_sqrtf(rbf(rbf(var) + 1e-5f)));
  Stats s; s.m = rbf(mean); s.rden = rcp(den); return s;
}
__device__ __forceinline__ float stats_reduce(v4u v) {
  float t = trsum4(asf4(v));
  t += dpp<ROR8>(t); t += dpp<ROR4>(t); t += dpp<XOR2>(t); t += dpp<XOR1>(t);
  return t;
}

#define STAMP(k) do { unsigned keep_; unsigned long long t_; const unsigned k_ = (k); \
    asm volatile("s_memrealtime %0\n\ts_waitcnt lgkmcnt(0)" : "=s"(t_)); const unsigned tl_ = (unsigned)t_; \
    asm volatile("s_mov_b32 %1, m0\n\ts_mov_b32 m0, %3\n\ts_nop 0\n\tv_writelane_b32 %0, %2, m0\n\ts_mov_b32 m0, %1" \
                 : "+v"(stv_), "=&s"(keep_) : "s"(tl_), "s"(k_)); } while (0)
#define MIMIC 1
#define DLY_H1 6
#define DLY_H2 10
#define DLY_H3 12
#define DLY_H4 46
#define DLY_H5 40
#define DLY_H6R 14
#define DLY_H6F 42
#define DLY_H6I 90
#define PSLEEP 1
template <int N> __device__ __forceinline__ void delay() { if constexpr (N > 0) __builtin_amdgcn_s_sleep(N); }

#define POLL(LOADS, MAXV) do { LOADS; unsigned sp_ = 0; \
    while (__builtin_expect(__builtin_amdgcn_ballot_w64((MAXV) == SENT) != 0ull, 0)) { if (++sp_ > SPIN_CAP) break; __builtin_amdgcn_s_sleep(PSLEEP); CFENCE(); LOADS; } } while (0)

__global__ __launch_bounds__(256, 2) void fused_decode(const u16* xin, const u16* kc, const u16* vc, const u16* wq, const u16* wo,
                                                        const u16* w1, const u16* b1, const u16* w2, const u16* b2, u16* out,
                                                        unsigned char* ws) {
  extern __shared__ __attribute__((aligned(16))) unsigned char smem[];
  LAS unsigned char* L = (LAS unsigned char*)smem;
  const unsigned tid = threadIdx.x, lane = tid & 63, g = lane >> 4, r = lane & 15;
  const unsigned w = __builtin_amdgcn_readfirstlane(tid >> 6);
  const unsigned bid = blockIdx.x, xcd = bid & 7, slot = bid >> 3, c = xcd * 32 + slot;
  const unsigned kvh = c >> 4, ii = c & 15;
  unsigned stv_ = 0;
  STAMP(0);
  const unsigned long long cyc0_ = __builtin_amdgcn_s_memtime();
  const unsigned cb = ii >> 2, kq = ii & 3;
  const unsigned ab = ii >> 3, asl = ii & 7;
  const unsigned rb = ii & 3, cb3 = ii >> 2;
  const unsigned fi = xcd * 18 + slot, ri = (slot - 18) * 8 + xcd;
  const bool isF = slot < 18, isR = slot >= 18 && ri < NR;
  const unsigned rep = xcd + ((isF || isR) ? 0u : 8u);
  const unsigned pc = lane & 7;
  const unsigned hu = ((r >> 1) & 4) * 4 + 4 * g + ((r >> 1) & 3);

  const rsrc_t rs = __builtin_amdgcn_make_buffer_rsrc((void*)ws, 0, (int)(WS_USED), 0x00020000);
  const v2u ep = ld64(rs, 0, WS_E);
  const rsrc_t rx = __builtin_amdgcn_make_buffer_rsrc((void*)xin, 0, 2 * D * 2, 0x00020000);
  v4u xf[5], xs[3];
  {
    const unsigned v = ((r & 1) * D + 144 * kq + 8 * g) * 2;
#pragma unroll
    for (int s = 0; s < 4; ++s) xf[s] = __builtin_amdgcn_raw_buffer_load_b128(rx, (int)(v + 64 * s), 0, 0);
    xf[4] = __builtin_amdgcn_raw_buffer_load_b128(rx, (int)(g < 2 ? v + 256 : OOB), 0, 0);
    const unsigned v2 = (((lane >> 1) & 1) * D + 16 * (lane >> 2) + 8 * (lane & 1)) * 2;
    xs[0] = __builtin_amdgcn_raw_buffer_load_b128(rx, (int)v2, 0, 0);
    xs[1] = __builtin_amdgcn_raw_buffer_load_b128(rx, (int)(v2 + 512), 0, 0);
    xs[2] = __builtin_amdgcn_raw_buffer_load_b128(rx, (int)(lane < 16 ? v2 + 1024 : OOB), 0, 0);
  }
  const rsrc_t rb2 = __builtin_amdgcn_make_buffer_rsrc((void*)b2, 0, D * 2, 0x00020000);
  const rsrc_t rb1 = __builtin_amdgcn_make_buffer_rsrc((void*)b1, 0, FF * 2, 0x00020000);
  const v2u xr0 = __builtin_amdgcn_raw_buffer_load_b64(rx, (int)(isR ? ((pc >> 2) * D + 16 * ri + 4 * (pc & 3)) * 2 : OOB), 0, 0);
  const v2u b2r = __builtin_amdgcn_raw_buffer_load_b64(rb2, (int)(isR ? (16 * ri + 4 * (pc & 3)) * 2 : OOB), 0, 0);
  const unsigned b1r = __builtin_amdgcn_raw_buffer_load_b16(rb1, (int)(isF ? (32 * fi + hu) * 2 : OOB), 0, 0);
  CFENCE();
  {
    const unsigned rowl = lane >> 3, s16 = lane & 7;
    const unsigned fsw = ((lane >> 4) & 1) | ((w & 1) << 1);
    const unsigned chunk = ((((s16 >> 1) ^ fsw) << 1) | (s16 & 1));
    const unsigned voff = (144 * kq + 8 * w + rowl) * 8192 + (kvh * 256 + 64 * cb) * 2 + chunk * 16;
#pragma unroll
    for (int k = 0; k < 4; ++k) glds(wq, voff + k * 32 * 8192, L_WQ + (w + 4 * k) * 1024);
    if (w < 2) glds(wq, voff + 4 * 32 * 8192, L_WQ + (w + 16) * 1024);
  }
  {
    const v4u ones = {SENT, SENT, SENT, SENT};
#pragma unroll
    for (int k = 0; k < 3; ++k) st128(rs, tid * 16 + k * 4096, WS_CB + c * CLR_PER_CU, ones);
  }
  v4u kf[8];
  {
    const u16* Kb = kc + ((size_t)(ab * 16 + kvh) * SK + (SK - WIN) + asl * 128 + w * 32) * DH;
    const rsrc_t rk = __builtin_amdgcn_make_buffer_rsrc((void*)Kb, 0, 32 * DH * 2, 0x00020000);
#pragma unroll
    for (int j = 0; j < 8; ++j) kf[j] = __builtin_amdgcn_raw_buffer_load_b128(rk, (int)(r * 256 + g * 16), (j >> 2) * 4096 + (j & 3) * 64, AUX_NT);
  }
  CFENCE();
  const unsigned q4 = r >> 2, p4 = r & 3;
  const unsigned jm = ((g & 1) << 1) | (g >> 1);
  unsigned vXf = (g >> 1) * 128 + (r & 1) * 32 + (g & 1) * 16;
  unsigned vXf4 = g < 2 ? vXf + 1024 : OOB;
  unsigned vXs = lane < NR ? lane * 128 : OOB;
  unsigned vRec = r < 2 ? r * 64 + g * 16 : OOB;
  unsigned vRed = (lane >> 3) * 128 + pc * 16;
  unsigned vRed4 = lane < 32 ? vRed + 4096 : OOB;
  unsigned vQ = (g * 16 + 4 * w + q4) * 128 + ab * 64 + p4 * 16;
  unsigned vO = (lane >> 3) * 1024 + pc * 16, vML = (lane >> 3) * 128;
  unsigned vXo = (lane >> 3) * X_REP + pc * 8;
  unsigned vSo = (pc & 3) == 0 ? (lane >> 3) * X_REP + (pc >> 2) * 8 : OOB;
  unsigned aWQ = L_WQ + (8 * g + q4) * 128 + ((w ^ (((q4 >> 1) & 1) | ((g & 1) << 1))) * 32) + 8 * p4;
  unsigned aWQ4 = g < 2 ? aWQ + 4 * 4096 : aWQ;
  unsigned aQIw = L_QI + (64 * w + 4 * r + jm) * 2, aQIr = L_QI + ((r & 1) * 128 + 8 * g) * 2;
  unsigned aV = L_V + w * 8192 + (4 * g + q4) * 256 + (4 * (g & 1) + q4) * 32 + 8 * p4;
  unsigned aOWw = g == 0 ? L_OW + (w * 128 + r) * 8 : L_DUM + lane * 8;
  unsigned aMw = (g == 0 && r < 2) ? L_ML + w * 16 + r * 4 : L_DUM + 1024 + lane * 4;
  unsigned aLw = lane == 0 ? L_ML + w * 16 + 8 : L_DUM + 1280 + lane * 8;
  unsigned aOWr = L_OW + (64 * (w & 1) + lane) * 8 + (w >> 1) * 4;
  unsigned aMr = L_ML + (w >> 1) * 4;
  unsigned aOIw = L_OI + ((w >> 1) * 64 + 32 * (w & 1) + 8 * (pc & 3) + 4 * (pc >> 2) + jm) * 2, aOIr = L_OI + ((r & 1) * 64 + 8 * g) * 2;
  unsigned aWO = L_WO + (4 * g + q4) * 288 + 32 * w + 8 * p4;
  unsigned aWO8 = L_WO + (4 * g + q4) * 288 + 32 * 8 + 8 * p4;
  unsigned aRSw = L_RS + (w * 32 + pc * 4 + jm) * 4, aRSr = L_RS + pc * 16;
  unsigned aW1 = L_W1 + w * 9216 + (8 * g + q4) * 64 + 8 * p4 + (g & 1) * 32;
  unsigned aW14 = g < 2 ? aW1 + 4 * 2048 : aW1;
  unsigned aW2 = L_W2 + w * 9216 + (4 * g + q4) * 288 + 8 * p4;
  unsigned aRDw = r < 3 ? L_RD + (w * 3 + r) * 128 + g * 16 : L_DUM + lane * 16;
  unsigned aRDr = L_RD + (r & 1) * 128 + hu * 4;
  PIN(vXf); PIN(vXf4); PIN(vXs); PIN(vRec); PIN(vRed); PIN(vRed4); PIN(vQ); PIN(vO); PIN(vML); PIN(vXo); PIN(vSo);
  PIN(aWQ); PIN(aWQ4); PIN(aQIw); PIN(aQIr); PIN(aV); PIN(aOWw); PIN(aMw); PIN(aLw); PIN(aOWr); PIN(aMr); PIN(aOIw); PIN(aOIr);
  PIN(aWO); PIN(aWO8); PIN(aRSw); PIN(aRSr); PIN(aW1); PIN(aW14); PIN(aW2); PIN(aRDw); PIN(aRDr);
  Stats st;
  {
    float sa = 0.f, sq = 0.f;
#pragma unroll
    for (int k = 0; k < 3; ++k) {
      sa = dot2(xs[k].x, ONES, sa); sa = dot2(xs[k].y, ONES, sa); sa = dot2(xs[k].z, ONES, sa); sa = dot2(xs[k].w, ONES, sa);
      sq = dot2(xs[k].x, xs[k].x, sq); sq = dot2(xs[k].y, xs[k].y, sq); sq = dot2(xs[k].z, xs[k].z, sq); sq = dot2(xs[k].w, xs[k].w, sq);
    }
    float t = t32_sum(sa, sq);
    { auto s = __builtin_amdgcn_permlane16_swap(asu(t), asu(t), false, false); unsigned a = s[0], b = s[1]; t = asf(a) + asf(b); }
    t += dpp<ROR8>(t); t += dpp<ROR4>(t); t += dpp<XOR1>(t);
    st = mkstats(rdl(t, ab * 2), rdl(t, 32 + ab * 2));
  }
  float xres[4], b2v[4];
  xres[0] = bflo(xr0.x); xres[1] = bfhi(xr0.x); xres[2] = bflo(xr0.y); xres[3] = bfhi(xr0.y);
  b2v[0] = bflo(b2r.x); b2v[1] = bfhi(b2r.x); b2v[2] = bflo(b2r.y); b2v[3] = bfhi(b2r.y);
  float b1v = bflo(b1r);
  PIN(st.m); PIN(st.rden); PIN(b1v); PIN(xres[0]); PIN(xres[1]); PIN(xres[2]); PIN(xres[3]); PIN(b2v[0]); PIN(b2v[1]); PIN(b2v[2]); PIN(b2v[3]);
  const unsigned tagl = ep.x + 1u, tagh = ep.y + (tagl == 0u ? 1u : 0u);
  STAMP(1);
  VMWAIT(8);
  BAR();
  if (tid == 0) st64(rs, c * 128, WS_H0, v2u{tagl, tagh});
  STAMP(2);
  v4u qv, vf[8];
  float cs = 0.f, cs1 = 0.f;

  {
    v4u wf[5];
#pragma unroll
    for (int s = 0; s < 4; ++s) wf[s] = trfrag(L, aWQ + s * 4096, aWQ + s * 4096 + 512);
    wf[4] = trfrag(L, aWQ4, aWQ4 + 512);
    if (r == 2) {
#pragma unroll
      for (int s = 0; s < 4; ++s) xf[s] = v4u{ONES, ONES, ONES, ONES};
      if (g < 2) xf[4] = v4u{ONES, ONES, ONES, ONES};
    }
    STAMP(3);
    f32x4 acc = {0.f, 0.f, 0.f, 0.f};
#pragma unroll
    for (int s = 0; s < 5; ++s) acc = mfma(wf[s], xf[s], acc);
    const unsigned vq0 = r < 3 ? r * 128 + g * 32 : OOB, sq0 = Q0_OFF + kvh * Q0_G + (kq * 16 + 4 * cb + w) * 512;
    st128(rs, vq0, sq0, v4u{asu(acc[0]), tagl, asu(acc[1]), tagl});
    st128(rs, vq0 + 16, sq0, v4u{asu(acc[2]), tagl, asu(acc[3]), tagl});
  }
  STAMP(4);
  {
    unsigned ln = lane; OPAQUE(ln);
    const u16* Vb = vc + ((size_t)(ab * 16 + kvh) * SK + (SK - WIN) + asl * 128 + w * 32) * DH;
    const unsigned gg = ln >> 4, rr = ln & 15;
    const unsigned v0 = gg * 256 + ((rr ^ (gg << 1)) * 16), v1 = (4 + gg) * 256 + ((rr ^ ((4 + gg) << 1)) * 16);
#pragma unroll
    for (int k = 0; k < 4; ++k) { glds(Vb, v0 + k * 2048, L_V + w * 8192 + k * 2048); glds(Vb, v1 + k * 2048, L_V + w * 8192 + k * 2048 + 1024); }
    STAMP(35);
    VMWAIT(3);
    const unsigned vq = (gg * 16 + 4 * w + ((ln >> 2) & 3)) * 512 + (ln & 3) * 32, sq = Q0_OFF + kvh * Q0_G;
    v4u qa, qb, ca, cb2;
    for (unsigned sp = 0;; ++sp) {
      CFENCE();
      const v2u t0 = ld64(rs, (w * 64 + ln) * 128, WS_H0);
      qa = ld128(rs, vq + ab * 128, sq); qb = ld128(rs, vq + ab * 128 + 16, sq);
      ca = ld128(rs, vq + 256, sq); cb2 = ld128(rs, vq + 272, sq);
      const bool ok = t0.x == tagl && t0.y == tagh && qa.y == tagl && qa.w == tagl && qb.y == tagl && qb.w == tagl &&
                      ca.y == tagl && ca.w == tagl && cb2.y == tagl && cb2.w == tagl;
      if (__all(ok) || sp > SPIN_CAP) break;
      __builtin_amdgcn_s_sleep(2);
    }
    STAMP(5);
    {
      const u16* Wb = wo + (size_t)(kvh * 256 + 64 * rb) * D + 144 * cb3;
#pragma unroll
      for (int k = 0; k < 5; ++k) {
        const unsigned piece = w + 4 * k;
        if (piece < 18) { const unsigned n = 64 * piece + ln, row = n / 18, ch = n - row * 18; glds(Wb, row * 1152 + ch * 16, L_WO + piece * 1024); }
      }
    }
    {
      unsigned a = aV; OPAQUE(a);
#pragma unroll
      for (int dt = 0; dt < 8; ++dt) vf[dt] = trfrag(L, a ^ (dt * 32), (a ^ (dt * 32)) + 16 * 256);
    }
    qv = v4u{qa.x, qa.z, qb.x, qb.z};
    cs = trsum4(f32x4{asf(ca.x), asf(ca.z), asf(cb2.x), asf(cb2.z)});
  }

#pragma unroll 1
  for (int lay = 0; lay < 2; ++lay) {
    {
      STAMP(6 + lay * 16);
      const float q = trsum4(asf4(qv));
      const unsigned qb = pk((q - st.m * cs) * st.rden, 0.f);
      *(LAS u16*)(L + aQIw) = (u16)qb;
    }
    BAR();
    {
      v4u qf[4];
#pragma unroll
      for (int s = 0; s < 4; ++s) qf[s] = *(LAS v4u*)(L + aQIr + 64 * s);
      f32x4 s0 = {0.f, 0.f, 0.f, 0.f}, s1 = {0.f, 0.f, 0.f, 0.f};
#pragma unroll
      for (int s = 0; s < 4; ++s) { s0 = mfma(kf[s], qf[s], s0); s1 = mfma(kf[4 + s], qf[s], s1); }
      float sc[8];
      {
        const float SC = 0.08838834764831845f;
        const unsigned u0 = pko(s0[0], s0[1]), u1 = pko(s0[2], s0[3]), u2 = pko(s1[0], s1[1]), u3 = pko(s1[2], s1[3]);
        const unsigned z0 = pko(bflo(u0) * SC, bfhi(u0) * SC), z1 = pko(bflo(u1) * SC, bfhi(u1) * SC);
        const unsigned z2 = pko(bflo(u2) * SC, bfhi(u2) * SC), z3 = pko(bflo(u3) * SC, bfhi(u3) * SC);
        sc[0] = bflo(z0); sc[1] = bfhi(z0); sc[2] = bflo(z1); sc[3] = bfhi(z1); sc[4] = bflo(z2); sc[5] = bfhi(z2); sc[6] = bflo(z3); sc[7] = bfhi(z3);
      }
      constexpr float EK = LOG2E;
      float mx = fmx3(fmx3(sc[0], sc[1], sc[2]), fmx3(sc[3], sc[4], sc[5]), fmx(sc[6], sc[7]));
      mx = x16_max(mx); mx = x32_max(mx);
      const float mb = -mx * EK;
      float e[8];
#pragma unroll
      for (int j = 0; j < 8; ++j) e[j] = ex2(__builtin_fmaf(sc[j], EK, mb));
      v4u pb; pb.x = pk(e[0], e[1]); pb.y = pk(e[2], e[3]); pb.z = pk(e[4], e[5]); pb.w = pk(e[6], e[7]);
      f32x4 oa[8];
#pragma unroll
      for (int dt = 0; dt < 8; ++dt) oa[dt] = mfma(pb, vf[dt], f32x4{0.f, 0.f, 0.f, 0.f});
      const f32x4 la = mfma(pb, v4u{ONES, ONES, ONES, ONES}, f32x4{0.f, 0.f, 0.f, 0.f});
#pragma unroll
      for (int dt = 0; dt < 8; ++dt) KEEP4(oa[dt]);
      KEEP4(la);
      *(LAS float*)(L + aMw) = mx * (EK / LOG2E);
#pragma unroll
      for (int dt = 0; dt < 8; ++dt) { v2u t; t.x = asu(oa[dt][0]); t.y = asu(oa[dt][1]); *(LAS v2u*)(L + aOWw + dt * 128) = t; }
      { v2u t; t.x = asu(la[0]); t.y = asu(la[1]); *(LAS v2u*)(L + aLw) = t; }
    }
    BAR();
    {
      const unsigned head = w >> 1;
      float m[4], l[4], o[4];
#pragma unroll
      for (int k = 0; k < 4; ++k) {
        m[k] = *(LAS float*)(L + aMr + k * 16);
        l[k] = *(LAS float*)(L + aMr + k * 16 + 8);
        o[k] = *(LAS float*)(L + aOWr + k * 1024);
      }
      const float M = fmx3(fmx(m[0], m[1]), m[2], m[3]);
      const float mb = -M * LOG2E;
      float f[4];
#pragma unroll
      for (int k = 0; k < 4; ++k) f[k] = ex2(__builtin_fmaf(m[k], LOG2E, mb));
      const float os = __builtin_fmaf(f[0], o[0], f[1] * o[1]) + __builtin_fmaf(f[2], o[2], f[3] * o[3]);
      const float lsum = __builtin_fmaf(f[0], l[0], f[1] * l[1]) + __builtin_fmaf(f[2], l[2], f[3] * l[3]);
      const unsigned sidx = (ab * 8 + asl) * 2 + head;
      st32(rs, lane * 4, O_OFF + lay * O_L + kvh * O_G + sidx * 512 + (w & 1) * 256, asu(os));
      v2u ml; ml.x = asu(M); ml.y = asu(lsum);
      st64(rs, ((w & 1) == 0 && lane == 0) ? 0u : OOB, ML_OFF + lay * ML_L + kvh * ML_G + sidx * 64, ml);
    }
    STAMP(7 + lay * 16);
    {
      v4u wof[6];
      const unsigned head = rb >> 1, b = w >> 1, sub = w & 1;
      v4u ov; v2u ml;
      delay<DLY_H2>();
      const unsigned soO = O_OFF + lay * O_L + kvh * O_G + (b * 16 + head) * 512 + (rb & 1) * 256 + sub * 128;
      const unsigned soM = ML_OFF + lay * ML_L + kvh * ML_G + (b * 16 + head) * 64;
      POLL(ov = ld128(rs, vO, soO); ml = ld64(rs, vML, soM), umax3(umax4(0u, ov), ml.x, ml.y));
      STAMP(8 + lay * 16);
      const float ms = asf(ml.x);
      float M = fmx(ms, dpp<ROR8>(ms)); M = x16_max(M); M = x32_max(M);
      const float f = ex2((ms - M) * LOG2E);
      float lsum = f * asf(ml.y);
      f32x4 o = asf4(ov) * f;
      lsum += dpp<ROR8>(lsum);
      float t = trsum4(o);
      { auto s = __builtin_amdgcn_permlane16_swap(asu(lsum), asu(lsum), false, false); unsigned x = s[0], y = s[1]; lsum = asf(x) + asf(y); }
      t += dpp<ROR8>(t);
      { auto s = __builtin_amdgcn_permlane32_swap(asu(lsum), asu(lsum), false, false); unsigned x = s[0], y = s[1]; lsum = asf(x) + asf(y); }
      const unsigned ob = pk(t * rcp(lsum), 0.f);
      *(LAS u16*)(L + aOIw) = (u16)ob;
      if (lay == 0) VMWAIT(0);
      BAR();
      const v4u of0 = *(LAS v4u*)(L + aOIr), of1 = *(LAS v4u*)(L + aOIr + 64);
      wof[0] = trfrag(L, aWO, aWO + 16 * 288); wof[2] = trfrag(L, aWO + 128, aWO + 128 + 16 * 288); wof[4] = trfrag(L, aWO8, aWO8 + 16 * 288);
      wof[1] = trfrag(L, aWO + 32 * 288, aWO + 48 * 288); wof[3] = trfrag(L, aWO + 128 + 32 * 288, aWO + 128 + 48 * 288);
      wof[5] = trfrag(L, aWO8 + 32 * 288, aWO8 + 48 * 288);
      const unsigned so = XP_OFF + lay * XP_L + 9 * cb3 * XP_R + (kvh * 4 + rb) * 128;
      f32x4 acc[3];
#pragma unroll
      for (int k = 0; k < 3; ++k) acc[k] = mfma(wof[2 * k], of0, f32x4{0.f, 0.f, 0.f, 0.f});
#pragma unroll
      for (int k = 0; k < 3; ++k) acc[k] = mfma(wof[2 * k + 1], of1, acc[k]);
      st128(rs, vRec, so + w * XP_R, asu4(acc[0]));
      st128(rs, vRec, so + (w + 4) * XP_R, asu4(acc[1]));
      if (w == 0) st128(rs, vRec, so + 8 * XP_R, asu4(acc[2]));
    }
    STAMP(9 + lay * 16);
    if (lay == 0 && isF) {
      unsigned ln = lane; OPAQUE(ln);
      const unsigned s4 = ln & 3, chunk = (((s4 >> 1) ^ ((ln >> 5) & 1)) << 1) | (s4 & 1);
      const unsigned v1 = (144 * w + (ln >> 2)) * (FF * 2) + 64 * fi + chunk * 16;
#pragma unroll
      for (int k = 0; k < 9; ++k) glds(w1, v1 + k * 16 * (FF * 2), L_W1 + w * 9216 + k * 1024);
      const u16* W2b = w2 + (size_t)(32 * fi) * D + 144 * w;
#pragma unroll
      for (int k = 0; k < 9; ++k) { const unsigned n = 64 * k + ln, row = n / 18, ch = n - row * 18; glds(W2b, row * 1152 + ch * 16, L_W2 + w * 9216 + k * 1024); }
    }
    if (isR) {
      v4u pv[2];
      delay<DLY_H3>();
      const unsigned so = XP_OFF + lay * XP_L + ri * XP_R + w * 2048;
      POLL(pv[0] = ld128(rs, vRed, so); pv[1] = ld128(rs, vRed + 1024, so), umax4(umax4(0u, pv[0]), pv[1]));
      STAMP(10 + lay * 16);
      float t = trsum4(asf4(pv[0]) + asf4(pv[1]));
      t += dpp<ROR8>(t);
      *(LAS float*)(L + aRSw) = t;
      BAR();
      if (w == 0) {
        const f32x4 s0 = *(LAS f32x4*)(L + aRSr), s1 = *(LAS f32x4*)(L + aRSr + 128), s2 = *(LAS f32x4*)(L + aRSr + 256), s3 = *(LAS f32x4*)(L + aRSr + 384);
        const f32x4 s = (s0 + s1) + (s2 + s3);
        const unsigned y0 = pk(s[0], s[1]), y1 = pk(s[2], s[3]);
        v2u xo; xo.x = san(pk(xres[0] + bflo(y0), xres[1] + bfhi(y0))); xo.y = san(pk(xres[2] + bflo(y1), xres[3] + bfhi(y1)));
        st64(rs, vXo, X1_OFF + lay * X1_L + ri * 128, xo);
        xres[0] = bflo(xo.x); xres[1] = bfhi(xo.x); xres[2] = bflo(xo.y); xres[3] = bfhi(xo.y);
        float sa = (xres[0] + xres[1]) + (xres[2] + xres[3]);
        float sq = __builtin_fmaf(xres[0], xres[0], xres[1] * xres[1]) + __builtin_fmaf(xres[2], xres[2], xres[3] * xres[3]);
        sa += dpp<XOR1>(sa); sq += dpp<XOR1>(sq); sa += dpp<XOR2>(sa); sq += dpp<XOR2>(sq);
        v2u so2; so2.x = asu(sa); so2.y = asu(sq);
        st64(rs, vSo, XS1_OFF + lay * X1_L + ri * 128, so2);
      }
      STAMP(11 + lay * 16);
    }
    if (isF) {
      v4u a0[5], a1[5];
      delay<DLY_H4>();
      if (lay == 0) VMWAIT(0);
#pragma unroll
      for (int s = 0; s < 4; ++s) {
        a0[s] = trfrag(L, aW1 + s * 2048, aW1 + s * 2048 + 256);
        a1[s] = trfrag(L, (aW1 ^ 32) + s * 2048, (aW1 ^ 32) + s * 2048 + 256);
      }
      a0[4] = trfrag(L, aW14, aW14 + 256); a1[4] = trfrag(L, aW14 ^ 32, (aW14 ^ 32) + 256);
      v4u x1[5], s1v;
      const unsigned soS = XS1_OFF + lay * X1_L + rep * X_REP;
      {
        const unsigned so = X1_OFF + lay * X1_L + rep * X_REP + 9 * w * 128;
        POLL(
          for (int s = 0; s < 4; ++s) x1[s] = ld128(rs, vXf + s * 256, so);
          x1[4] = ld128(rs, vXf4, so); s1v = ld128(rs, vXs, soS),
          umax4(umax4(umax4(umax4(umax4(umax4(0u, x1[0]), x1[1]), x1[2]), x1[3]), x1[4]), s1v));
      }
      if (lay == 0 && r == 2) {
#pragma unroll
        for (int s = 0; s < 4; ++s) x1[s] = v4u{ONES, ONES, ONES, ONES};
        if (g < 2) x1[4] = v4u{ONES, ONES, ONES, ONES};
      }
      STAMP(12 + lay * 16);
      f32x4 h0 = {0.f, 0.f, 0.f, 0.f}, h1 = {0.f, 0.f, 0.f, 0.f};
#pragma unroll
      for (int s = 0; s < 5; ++s) { h0 = mfma(a0[s], x1[s], h0); h1 = mfma(a1[s], x1[s], h1); }
      v4u w2f[9];
#pragma unroll
      for (int t = 0; t < 9; ++t) w2f[t] = trfrag(L, aW2 + 32 * t, aW2 + 32 * t + 16 * 288);
      float sm, sr;
      {
        const float t = stats_reduce(s1v);
        auto s = __builtin_amdgcn_permlane32_swap(asu(t), asu(t), false, false);
        const unsigned x = s[0], y = s[1];
        const Stats z = mkstats(asf(x), asf(y));
        const float m0 = rdl(z.m, 0), m1 = rdl(z.m, 16), r0 = rdl(z.rden, 0), r1 = rdl(z.rden, 16);
        sm = (r & 1) ? m1 : m0; sr = (r & 1) ? r1 : r0;
      }
      *(LAS f32x4*)(L + aRDw) = h0;
      *(LAS f32x4*)(L + aRDw + 64) = h1;
      BAR();
      const float hs = (*(LAS float*)(L + aRDr) + *(LAS float*)(L + aRDr + 384)) + (*(LAS float*)(L + aRDr + 768) + *(LAS float*)(L + aRDr + 1152));
      if (lay == 0) {
        const unsigned ac = L_RD + 2 * 128 + hu * 4;
        cs1 = (*(LAS float*)(L + ac) + *(LAS float*)(L + ac + 384)) + (*(LAS float*)(L + ac + 768) + *(LAS float*)(L + ac + 1152));
      }
      const float pre = rbf((hs - sm * cs1) * sr);
      const float a = rbf(pre + b1v);
      const float u = a * __builtin_fmaf(a * a, 0.7978845608028654f * 0.044715f, 0.7978845608028654f);
      const float hv = a * rcp(1.0f + ex2(-2.0f * LOG2E * u));
      const unsigned hp = pk(hv, dpp<SHL2>(hv));
      v4u hb; hb.x = hp; hb.y = dppu<SHL4>(hp); hb.z = dppu<SHL8>(hp); hb.w = dppu<SHL12>(hp);
      const unsigned so = FP_OFF + lay * FP_L + 9 * w * FP_R + fi * 128;
      f32x4 acc[9];
#pragma unroll
      for (int t = 0; t < 9; ++t) acc[t] = mfma(w2f[t], hb, f32x4{0.f, 0.f, 0.f, 0.f});
      SBAR();
#pragma unroll
      for (int t = 0; t < 9; ++t) st128(rs, vRec, so + t * FP_R, asu4(acc[t]));
      STAMP(13 + lay * 16);
    }
    if (isR) {
      v4u pv[5];
      delay<DLY_H5>();
      const unsigned so = FP_OFF + lay * FP_L + ri * FP_R + w * 36 * 128;
      POLL(
        for (int k = 0; k < 4; ++k) pv[k] = ld128(rs, vRed + k * 1024, so);
        pv[4] = ld128(rs, vRed4, so),
        umax4(umax4(umax4(umax4(umax4(0u, pv[0]), pv[1]), pv[2]), pv[3]), pv[4]));
      STAMP(14 + lay * 16);
      float t = trsum4(((asf4(pv[0]) + asf4(pv[1])) + (asf4(pv[2]) + asf4(pv[3]))) + asf4(pv[4]));
      t += dpp<ROR8>(t);
      *(LAS float*)(L + aRSw) = t;
      BAR();
      if (w == 0) {
        const f32x4 s0 = *(LAS f32x4*)(L + aRSr), s1 = *(LAS f32x4*)(L + aRSr + 128), s2 = *(LAS f32x4*)(L + aRSr + 256), s3 = *(LAS f32x4*)(L + aRSr + 384);
        const f32x4 s = (s0 + s1) + (s2 + s3);
        const unsigned y0 = pk(s[0], s[1]), y1 = pk(s[2], s[3]);
        const unsigned z0 = pk(bflo(y0) + b2v[0], bfhi(y0) + b2v[1]), z1 = pk(bflo(y1) + b2v[2], bfhi(y1) + b2v[3]);
        v2u xo; xo.x = pk(xres[0] + bflo(z0), xres[1] + bfhi(z0)); xo.y = pk(xres[2] + bflo(z1), xres[3] + bfhi(z1));
        if (lay == 0) {
          xo.x = san(xo.x); xo.y = san(xo.y);
          st64(rs, vXo, X2_OFF + ri * 128, xo);
          st64(rs, vXo, X2_OFF + 8 * X_REP + ri * 128, xo);
          xres[0] = bflo(xo.x); xres[1] = bfhi(xo.x); xres[2] = bflo(xo.y); xres[3] = bfhi(xo.y);
          float sa = (xres[0] + xres[1]) + (xres[2] + xres[3]);
          float sq = __builtin_fmaf(xres[0], xres[0], xres[1] * xres[1]) + __builtin_fmaf(xres[2], xres[2], xres[3] * xres[3]);
          sa += dpp<XOR1>(sa); sq += dpp<XOR1>(sq); sa += dpp<XOR2>(sa); sq += dpp<XOR2>(sq);
          v2u so2; so2.x = asu(sa); so2.y = asu(sq);
          st64(rs, vSo, XS2_OFF + ri * 128, so2);
          st64(rs, vSo, XS2_OFF + 8 * X_REP + ri * 128, so2);
        } else if (lane < 8) {
          *(GAS v2u*)(out + (pc >> 2) * D + 16 * ri + 4 * (pc & 3)) = xo;
        }
      }
      STAMP(15 + lay * 16);
    }
    if (lay == 0) {
      v4u wf[5], xg[5], sv;
#pragma unroll
      for (int s = 0; s < 4; ++s) wf[s] = trfrag(L, aWQ + s * 4096, aWQ + s * 4096 + 512);
      wf[4] = trfrag(L, aWQ4, aWQ4 + 512);
      if (isR) delay<DLY_H6R>(); else if (isF) delay<DLY_H6F>(); else delay<DLY_H6I>();
      const unsigned so = X2_OFF + rep * X_REP + 9 * kq * 128;
      POLL(
        for (int s = 0; s < 4; ++s) xg[s] = ld128(rs, vXf + s * 256, so);
        xg[4] = ld128(rs, vXf4, so); sv = ld128(rs, vXs, XS2_OFF + rep * X_REP),
        umax4(umax4(umax4(umax4(umax4(umax4(0u, xg[0]), xg[1]), xg[2]), xg[3]), xg[4]), sv));
      STAMP(19);
      f32x4 acc = {0.f, 0.f, 0.f, 0.f};
#pragma unroll
      for (int s = 0; s < 5; ++s) acc = mfma(wf[s], xg[s], acc);
      st128(rs, vRec, Q1_OFF + kvh * Q_G + (kq * 16 + 4 * cb + w) * 128, asu4(acc));
      STAMP(20);
      {
        unsigned a = aV; OPAQUE(a);
#pragma unroll
        for (int dt = 0; dt < 8; ++dt) vf[dt] = trfrag(L, a ^ (dt * 32), (a ^ (dt * 32)) + 16 * 256);
      }
      const float t = stats_reduce(sv);
      st = mkstats(rdl(t, 16 * ab), rdl(t, 32 + 16 * ab));
      STAMP(21);
      delay<DLY_H1>();
      POLL(qv = ld128(rs, vQ, Q1_OFF + kvh * Q_G), umax4(0u, qv));
    }
  }
  if (c == 0 && tid == 0) st64(rs, 0, WS_E, v2u{tagl, tagh});
  STAMP(62);
  { const unsigned long long cyc1_ = __builtin_amdgcn_s_memtime(); if (lane == 61) stv_ = (unsigned)(cyc1_ - cyc0_); }
  ((GAS unsigned*)(ws + WS_DBG))[(bid * 4 + w) * 64 + lane] = stv_;
  if ((bid == 0 || bid == 150 || bid == 255) && w == 0) {
    const unsigned t0 = __builtin_amdgcn_readlane(stv_, 0);
    unsigned a2 = __builtin_amdgcn_readlane(stv_, 2), a4 = __builtin_amdgcn_readlane(stv_, 4), a5 = __builtin_amdgcn_readlane(stv_, 5), a7 = __builtin_amdgcn_readlane(stv_, 7), a9 = __builtin_amdgcn_readlane(stv_, 9), a19 = __builtin_amdgcn_readlane(stv_, 19), a23 = __builtin_amdgcn_readlane(stv_, 23), a25 = __builtin_amdgcn_readlane(stv_, 25), a62 = __builtin_amdgcn_readlane(stv_, 62);
    if (lane == 0) printf("bid %u: s2 %u s4 %u s5 %u s7 %u s9 %u s19 %u s23 %u s25 %u s62 %u (x10ns)\n", bid, a2 - t0, a4 - t0, a5 - t0, a7 - t0, a9 - t0, a19 - t0, a23 - t0, a25 - t0, a62 - t0);
  }
}

}

extern "C" void kernel_launch(void* const* d_in, const int* in_sizes, int n_in, void* d_out, int out_size, void* d_ws, size_t ws_size,
                              hipStream_t stream) {
  (void)in_sizes; (void)n_in; (void)out_size; (void)ws_size;
  (void)hipFuncSetAttribute((const void*)fk::fused_decode, hipFuncAttributeMaxDynamicSharedMemorySize, (int)fk::L_END);
  fk::fused_decode<<<256, 256, fk::L_END, stream>>>((const u16*)d_in[0], (const u16*)d_in[1], (const u16*)d_in[2], (const u16*)d_in[3],
                                            (const u16*)d_in[4], (const u16*)d_in[6], (const u16*)d_in[7], (const u16*)d_in[8],
                                            (const u16*)d_in[9], (u16*)d_out, (unsigned char*)d_ws);
}
```

```cpp
#include <hip/hip_runtime.h>
#include <stdint.h>

typedef unsigned short u16;
typedef unsigned v4u __attribute__((ext_vector_type(4)));
typedef unsigned v2u __attribute__((ext_vector_type(2)));
typedef float f32x4 __attribute__((ext_vector_type(4)));
typedef __bf16 bf16x8 __attribute__((ext_vector_type(8)));
typedef __bf16 bf16x2 __attribute__((ext_vector_type(2)));
typedef short s16x4 __attribute__((ext_vector_type(4)));
#define LAS __attribute__((address_space(3)))
#define GAS __attribute__((address_space(1)))

#define STAMPS 0

#pragma clang diagnostic ignored "-Wint-to-pointer-cast"
namespace fk {

constexpr int D = 576, FF = 4608, SK = 4096, WIN = 1024, DH = 128;
constexpr int NR = 36;

constexpr unsigned WS_E = 0, WS_H0 = 4096;
constexpr unsigned Q0_OFF = WS_H0 + 256 * 128, Q0_G = 64 * 512;
constexpr unsigned O_G = 16384, O_L = 16 * O_G;
constexpr unsigned ML_G = 2048, ML_L = 16 * ML_G;
constexpr unsigned XP_R = 64 * 128, XP_L = NR * XP_R;
constexpr unsigned X_REP = NR * 128, X1_L = 8 * X_REP;
constexpr unsigned WS_CB = Q0_OFF + 16 * Q0_G;
constexpr unsigned XP0_OFF = WS_CB;
constexpr unsigned X10_OFF = XP0_OFF + XP_L, XS10_OFF = X10_OFF + X1_L;
constexpr unsigned X2_OFF = XS10_OFF + X1_L, XS2_OFF = X2_OFF + 16 * X_REP;
constexpr unsigned CB_END = XS2_OFF + 16 * X_REP, CLR_PER_CU = 2048;
static_assert(CB_END <= WS_CB + 256 * CLR_PER_CU, "clear region");
constexpr unsigned Q_G = 8192, Q_L = 16 * Q_G;
constexpr unsigned FP_R = 144 * 128, FP_L = NR * FP_R;
constexpr unsigned O0_OFF = WS_CB + 256 * CLR_PER_CU, ML0_OFF = O0_OFF + O_L;
constexpr unsigned Q1_OFF = ML0_OFF + ML_L;
constexpr unsigned O1_OFF = Q1_OFF + Q_L, ML1_OFF = O1_OFF + O_L, XP1_OFF = ML1_OFF + ML_L;
constexpr unsigned X11_OFF = XP1_OFF + XP_L, XS11_OFF = X11_OFF + X1_L;
constexpr unsigned FP0_OFF = XS11_OFF + X1_L, FP1_OFF = FP0_OFF + FP_L;
constexpr unsigned WS_USED = FP1_OFF + FP_L;
constexpr unsigned WS_DBG = WS_USED;
constexpr unsigned OOB = 0x80000000u;

constexpr unsigned L_WQ = 0, L_V = 18432, L_WO = L_V + 32768, L_W1 = L_WO + 18432, L_W2 = L_W1 + 36864;
constexpr unsigned L_QI = L_W2 + 36864, L_OW = L_QI + 512, L_ML = L_OW + 4096, L_OI = L_ML + 64, L_RD = L_OI + 256;
constexpr unsigned L_RS = L_RD + 4 * 3 * 128, L_XG = L_RS + 512, L_ZERO = L_XG + 4 * 576, L_ST = L_ZERO + 128, L_END = L_ST + 16;
static_assert(L_END <= 163840, "LDS");

constexpr unsigned SENT = 0xFFFFFFFFu;
constexpr unsigned SPIN_CAP = 1u << 16;
constexpr int AUX_SC1 = 16, AUX_NT = 2;
constexpr unsigned ONES = 0x3F803F80u;

__device__ __forceinline__ float asf(unsigned u) { return __builtin_bit_cast(float, u); }
__device__ __forceinline__ unsigned asu(float f) { return __builtin_bit_cast(unsigned, f); }
__device__ __forceinline__ bf16x2 asbf2(unsigned u) { return __builtin_bit_cast(bf16x2, u); }
__device__ __forceinline__ unsigned pk(float lo, float hi) { bf16x2 v = {(__bf16)lo, (__bf16)hi}; return __builtin_bit_cast(unsigned, v); }
__device__ __forceinline__ float bflo(unsigned u) { return asf(u << 16); }
__device__ __forceinline__ float bfhi(unsigned u) { return asf(u & 0xFFFF0000u); }
__device__ __forceinline__ float rbf(float x) { return bflo(pk(x, x)); }
__device__ __forceinline__ unsigned san(unsigned u) { return u < 0xFFFFFFFEu ? u : 0xFFFFFFFEu; }
__device__ __forceinline__ f32x4 asf4(v4u v) { f32x4 o; o.x = asf(v.x); o.y = asf(v.y); o.z = asf(v.z); o.w = asf(v.w); return o; }
__device__ __forceinline__ v4u asu4(f32x4 v) { v4u o; o.x = asu(v.x); o.y = asu(v.y); o.z = asu(v.z); o.w = asu(v.w); return o; }
__device__ __forceinline__ unsigned um2(unsigned a, unsigned b) { return a > b ? a : b; }
__device__ __forceinline__ unsigned um3(unsigned a, unsigned b, unsigned c) { return um2(um2(a, b), c); }
__device__ __forceinline__ unsigned vmax1(v4u a) { return um2(um3(a.x, a.y, a.z), a.w); }
__device__ __forceinline__ unsigned vmax2(v4u a, v4u b) { return um3(um3(a.x, a.y, a.z), um3(a.w, b.x, b.y), um2(b.z, b.w)); }
__device__ __forceinline__ unsigned vmax5(const v4u (&a)[5]) {
  return um3(um3(um3(a[0].x, a[0].y, a[0].z), um3(a[0].w, a[1].x, a[1].y), um3(a[1].z, a[1].w, a[2].x)),
             um3(um3(a[2].y, a[2].z, a[2].w), um3(a[3].x, a[3].y, a[3].z), um3(a[3].w, a[4].x, a[4].y)), um2(a[4].z, a[4].w));
}
__device__ __forceinline__ f32x4 mfma(v4u a, v4u b, f32x4 c) {
  return __builtin_amdgcn_mfma_f32_16x16x32_bf16(__builtin_bit_cast(bf16x8, a), __builtin_bit_cast(bf16x8, b), c, 0, 0, 0);
}
__device__ __forceinline__ float dot2(unsigned a, unsigned b, float c) { return __builtin_amdgcn_fdot2_f32_bf16(asbf2(a), asbf2(b), c, false); }
template <int CTRL> __device__ __forceinline__ unsigned dppu(unsigned x) { return (unsigned)__builtin_amdgcn_mov_dpp((int)x, CTRL, 0xf, 0xf, true); }
template <int CTRL> __device__ __forceinline__ float dpp(float x) { return asf(dppu<CTRL>(asu(x))); }
constexpr int XOR1 = 0xB1, XOR2 = 0x4E, ROR4 = 0x124, ROR8 = 0x128, SHL2 = 0x102, SHL4 = 0x104, SHL8 = 0x108, SHL12 = 0x10C;
__device__ __forceinline__ float t32_sum(float a, float b) { auto s = __builtin_amdgcn_permlane32_swap(asu(a), asu(b), false, false); unsigned x = s[0], y = s[1]; return asf(x) + asf(y); }
__device__ __forceinline__ float t16_sum(float a, float b) { auto s = __builtin_amdgcn_permlane16_swap(asu(a), asu(b), false, false); unsigned x = s[0], y = s[1]; return asf(x) + asf(y); }
__device__ __forceinline__ float trsum4(f32x4 v) { return t16_sum(t32_sum(v.x, v.y), t32_sum(v.z, v.w)); }
__device__ __forceinline__ float fmx(float a, float b) { return __builtin_elementwise_maximum(a, b); }
__device__ __forceinline__ float fmx3(float a, float b, float c) { return fmx(fmx(a, b), c); }
__device__ __forceinline__ float x16_max(float x) { auto s = __builtin_amdgcn_permlane16_swap(asu(x), asu(x), false, false); unsigned a = s[0], b = s[1]; return fmx(asf(a), asf(b)); }
__device__ __forceinline__ float x32_max(float x) { auto s = __builtin_amdgcn_permlane32_swap(asu(x), asu(x), false, false); unsigned a = s[0], b = s[1]; return fmx(asf(a), asf(b)); }
__device__ __forceinline__ unsigned pko(float lo, float hi) { unsigned u = pk(lo, hi); asm("" : "+v"(u)); return u; }
#define KEEP4(x) asm volatile("" :: "v"(x))
__device__ __forceinline__ float ex2(float x) { return __builtin_amdgcn_exp2f(x); }
__device__ __forceinline__ float rcp(float x) { return __builtin_amdgcn_rcpf(x); }
__device__ __forceinline__ float rdl(float x, int l) { return asf((unsigned)__builtin_amdgcn_readlane((int)asu(x), l)); }
constexpr float LOG2E = 1.4426950408889634f;

#define BAR() asm volatile("s_waitcnt lgkmcnt(0)\n\ts_barrier" ::: "memory")
#define VMWAIT(n) asm volatile("s_waitcnt vmcnt(" #n ")" ::: "memory")
#define VMWAITN(n) asm volatile("s_waitcnt vmcnt(%0)" :: "n"(n) : "memory")
#define CFENCE() asm volatile("" ::: "memory")
#define OPAQUE(x) asm volatile("" : "+v"(x))
#define PIN(x) asm volatile("" : "+v"(x))
#define PINS(x) asm volatile("" : "+s"(x))
#define SBAR() __builtin_amdgcn_sched_barrier(0)

__device__ __forceinline__ void glds(const void* base, unsigned voff, unsigned ldsd) {
  unsigned keep;
  asm volatile("s_mov_b32 %0, m0\n\ts_mov_b32 m0, %3\n\ts_nop 0\n\tglobal_load_lds_dwordx4 %1, %2 nt\n\ts_mov_b32 m0, %0"
               : "=&s"(keep) : "v"(voff), "s"(base), "s"(ldsd) : "memory");
}
__device__ __forceinline__ v4u trfrag(unsigned a0, unsigned a1) {
  s16x4 x = __builtin_amdgcn_ds_read_tr16_b64_v4i16((LAS s16x4*)(a0));
  s16x4 y = __builtin_amdgcn_ds_read_tr16_b64_v4i16((LAS s16x4*)(a1));
  v2u ux = __builtin_bit_cast(v2u, x), uy = __builtin_bit_cast(v2u, y);
  v4u o; o.x = ux.x; o.y = ux.y; o.z = uy.x; o.w = uy.y; return o;
}
typedef __amdgpu_buffer_rsrc_t rsrc_t;
__device__ __forceinline__ v4u ld128(rsrc_t rs, unsigned v, unsigned s) { return __builtin_amdgcn_raw_buffer_load_b128(rs, (int)v, (int)s, AUX_SC1); }
__device__ __forceinline__ v2u ld64(rsrc_t rs, unsigned v, unsigned s) { return __builtin_amdgcn_raw_buffer_load_b64(rs, (int)v, (int)s, AUX_SC1); }
__device__ __forceinline__ void st128(rsrc_t rs, unsigned v, unsigned s, v4u d) { __builtin_amdgcn_raw_buffer_store_b128(d, rs, (int)v, (int)s, AUX_SC1); }
__device__ __forceinline__ void st64(rsrc_t rs, unsigned v, unsigned s, v2u d) { __builtin_amdgcn_raw_buffer_store_b64(d, rs, (int)v, (int)s, AUX_SC1); }
__device__ __forceinline__ void st32(rsrc_t rs, unsigned v, unsigned s, unsigned d) { __builtin_amdgcn_raw_buffer_store_b32(d, rs, (int)v, (int)s, AUX_SC1); }

struct Stats { float m, rden; };
__device__ __forceinline__ Stats mkstats(float sx, float sxx) {
  const float mean = sx * (1.0f / D);
  float var = __builtin_fmaf(-mean, mean, sxx * (1.0f / D));
  var = var > 0.f ? var : 0.f;
  const float den = rbf(__builtin_amdgcn_sqrtf(rbf(rbf(var) + 1e-5f)));
  Stats s; s.m = rbf(mean); s.rden = rcp(den); return s;
}
__device__ __forceinline__ float stats_reduce(v4u v) {
  float t = trsum4(asf4(v));
  t += dpp<ROR8>(t); t += dpp<ROR4>(t); t += dpp<XOR2>(t); t += dpp<XOR1>(t);
  return t;
}

#define STAMP(k) do {} while (0)
#define MIMIC 1
#define DLY_H1 2
#define DLY_H2 12
#define DLY_H3 12
#define DLY_H4 40
#define DLY_H5 40
#define DLY_H6R 14
#define DLY_H6F 42
#define DLY_H6I 90
#define PSLEEP 1
#define H1MODE 0
#define GAP_H1 6
#define W1EARLY 0
#define VEARLY 2
#define VLEFT 6
#define W2EARLY 9
template <int N> __device__ __forceinline__ void delay() { if constexpr (N > 0) __builtin_amdgcn_s_sleep(N); }

#define POLL(LOADS, MAXV) do { unsigned sp_ = 0; \
    do { CFENCE(); LOADS; if (__builtin_expect(__builtin_amdgcn_ballot_w64((MAXV) == SENT) == 0ull, 1)) break; __builtin_amdgcn_s_sleep(PSLEEP); } while (++sp_ <= SPIN_CAP); } while (0)

#define POLL2(T, RES, LOAD, BAD, GAP) do { T pa_, pb_; unsigned sp_ = 0; \
    pa_ = LOAD; __builtin_amdgcn_s_sleep(GAP); CFENCE(); pb_ = LOAD; \
    for (;;) { \
      { const T x_ = pa_; if (__builtin_amdgcn_ballot_w64(BAD) == 0ull) { RES = pa_; break; } } \
      CFENCE(); pa_ = LOAD; \
      { const T x_ = pb_; if (__builtin_amdgcn_ballot_w64(BAD) == 0ull || ++sp_ > SPIN_CAP) { RES = pb_; break; } } \
      CFENCE(); pb_ = LOAD; \
    } } while (0)
#define UNDEF5(x) do { asm volatile("" : "=v"(x[0]), "=v"(x[1]), "=v"(x[2]), "=v"(x[3]), "=v"(x[4])); } while (0)
#define POLLM(LOADS, BAD) do { unsigned sp_ = 0; \
    do { CFENCE(); LOADS; if (__builtin_expect(__builtin_amdgcn_ballot_w64(BAD) == 0ull, 1)) break; __builtin_amdgcn_s_sleep(PSLEEP); } while (++sp_ <= SPIN_CAP); } while (0)

__global__ __launch_bounds__(256, 2) void fused_decode(const u16* xin, const u16* kc, const u16* vc, const u16* wq, const u16* wo,
                                                        const u16* w1, const u16* b1, const u16* w2, const u16* b2, u16* out,
                                                        unsigned char* ws) {
  extern __shared__ __attribute__((aligned(16))) unsigned char smem[];
  const unsigned LB = (unsigned)(uintptr_t)(LAS unsigned char*)smem;
  const unsigned tid = threadIdx.x, lane = tid & 63, g = lane >> 4, r = lane & 15;
  const unsigned w = __builtin_amdgcn_readfirstlane(tid >> 6);
  const unsigned bid = blockIdx.x, xcd = bid & 7, slot = bid >> 3, c = xcd * 32 + slot;
  const unsigned kvh = c >> 4, ii = c & 15;
  STAMP(0);
  const unsigned cb = ii >> 2, kq = ii & 3;
  const unsigned ab = ii >> 3, asl = ii & 7;
  const unsigned rb = ii & 3, cb3 = ii >> 2;
  const unsigned fi = xcd * 18 + slot, ri = (slot - 18) * 8 + xcd;
  const bool isF = slot < 18, isR = slot >= 18 && ri < NR;
  const unsigned rep = xcd + ((isF || isR) ? 0u : 8u);
  const unsigned pc = lane & 7;
  const unsigned hu = ((r >> 1) & 4) * 4 + 4 * g + ((r >> 1) & 3);

  const rsrc_t rs = __builtin_amdgcn_make_buffer_rsrc((void*)ws, 0, (int)(WS_USED), 0x00020000);
  const rsrc_t rx = __builtin_amdgcn_make_buffer_rsrc((void*)xin, 0, 2 * D * 2, 0x00020000);
  v4u xf[5];
  {
    const unsigned v = ((r & 1) * D + 144 * kq + 8 * g) * 2;
#pragma unroll
    for (int s = 0; s < 4; ++s) xf[s] = __builtin_amdgcn_raw_buffer_load_b128(rx, (int)(v + 64 * s), 0, 0);
    xf[4] = __builtin_amdgcn_raw_buffer_load_b128(rx, (int)(g < 2 ? v + 256 : OOB), 0, 0);
  }
  const v2u ep = ld64(rs, 0, WS_E);
  const size_t kvbase = ((size_t)(ab * 16 + kvh) * SK + (SK - WIN) + asl * 128 + w * 32) * DH;
  v4u xs[3];
  v2u xr0, b2r;
  unsigned b1r;
  {
    const unsigned v2 = (((lane >> 1) & 1) * D + 16 * (lane >> 2) + 8 * (lane & 1)) * 2;
    xs[0] = __builtin_amdgcn_raw_buffer_load_b128(rx, (int)v2, 0, 0);
    xs[1] = __builtin_amdgcn_raw_buffer_load_b128(rx, (int)(v2 + 512), 0, 0);
    xs[2] = __builtin_amdgcn_raw_buffer_load_b128(rx, (int)(lane < 16 ? v2 + 1024 : OOB), 0, 0);
    const rsrc_t rb2 = __builtin_amdgcn_make_buffer_rsrc((void*)b2, 0, D * 2, 0x00020000);
    const rsrc_t rb1 = __builtin_amdgcn_make_buffer_rsrc((void*)b1, 0, FF * 2, 0x00020000);
    xr0 = __builtin_amdgcn_raw_buffer_load_b64(rx, (int)(isR ? ((pc >> 2) * D + 16 * ri + 4 * (pc & 3)) * 2 : OOB), 0, 0);
    b2r = __builtin_amdgcn_raw_buffer_load_b64(rb2, (int)(isR ? (16 * ri + 4 * (pc & 3)) * 2 : OOB), 0, 0);
    b1r = __builtin_amdgcn_raw_buffer_load_b16(rb1, (int)(isF ? (32 * fi + hu) * 2 : OOB), 0, 0);
  }
  CFENCE();
  {
    const unsigned rowl = lane >> 3, s16 = lane & 7;
    const unsigned fsw = ((lane >> 4) & 1) | ((w & 1) << 1);
    const unsigned chunk = ((((s16 >> 1) ^ fsw) << 1) | (s16 & 1));
    const unsigned voff = (144 * kq + 8 * w + rowl) * 8192 + (kvh * 256 + 64 * cb) * 2 + chunk * 16;
#pragma unroll
    for (int k = 0; k < 4; ++k) glds(wq, voff + k * 32 * 8192, LB + L_WQ + (w + 4 * k) * 1024);
    if (w < 2) glds(wq, voff + 4 * 32 * 8192, LB + L_WQ + (w + 16) * 1024);
  }
  {
    const unsigned sidx = (ab * 8 + asl) * 2 + (w >> 1);
    st32(rs, lane * 4, O0_OFF + kvh * O_G + sidx * 512 + (w & 1) * 256, SENT);
    st64(rs, ((w & 1) == 0 && lane == 0) ? 0u : OOB, ML0_OFF + kvh * ML_G + sidx * 64, v2u{SENT, SENT});
  }
  v4u kf[8];
  {
    const rsrc_t rk = __builtin_amdgcn_make_buffer_rsrc((void*)(kc + kvbase), 0, 32 * DH * 2, 0x00020000);
#pragma unroll
    for (int j = 0; j < 8; ++j) kf[j] = __builtin_amdgcn_raw_buffer_load_b128(rk, (int)(r * 256 + g * 16), (j >> 2) * 4096 + (j & 3) * 64, AUX_NT);
  }
  const u16* Vb = vc + kvbase;
  const unsigned vV0 = g * 256 + ((r ^ (g << 1)) * 16), vV1 = (4 + g) * 256 + ((r ^ ((4 + g) << 1)) * 16);
#pragma unroll
  for (int k = 0; k < VEARLY; ++k) { glds(Vb, vV0 + k * 2048, LB + L_V + w * 8192 + k * 2048); glds(Vb, vV1 + k * 2048, LB + L_V + w * 8192 + k * 2048 + 1024); }
  CFENCE();
  const unsigned q4 = r >> 2, p4 = r & 3;
  const unsigned aWQ = LB + L_WQ + (8 * g + q4) * 128 + ((w ^ (((q4 >> 1) & 1) | ((g & 1) << 1))) * 32) + 8 * p4;
  const unsigned aWQ4 = g < 2 ? aWQ + 4 * 4096 : aWQ;
  STAMP(1);
  VMWAITN(2 + 8 + 2 * VEARLY);
  BAR();
  const unsigned tagl = ep.x + 1u, tagh = ep.y + (tagl == 0u ? 1u : 0u);
  STAMP(2);
  v4u qv, vf[8];
  if (tid < 8) *(LAS v4u*)(LB + L_ZERO + tid * 16) = v4u{0u, 0u, 0u, 0u};
  v4u hA[4] = {{0u, 0u, 0u, 0u}, {0u, 0u, 0u, 0u}, {0u, 0u, 0u, 0u}, {0u, 0u, 0u, 0u}};
  float cs = 0.f, cs1 = 0.f;

  {
    v4u wf[5];
#pragma unroll
    for (int s = 0; s < 4; ++s) wf[s] = trfrag(aWQ + s * 4096, aWQ + s * 4096 + 512);
    wf[4] = trfrag(aWQ4, aWQ4 + 512);
    if (r == 2) {
#pragma unroll
      for (int s = 0; s < 4; ++s) xf[s] = v4u{ONES, ONES, ONES, ONES};
      if (g < 2) xf[4] = v4u{ONES, ONES, ONES, ONES};
    }
    STAMP(3);
    f32x4 acc = {0.f, 0.f, 0.f, 0.f};
#pragma unroll
    for (int s = 0; s < 5; ++s) acc = mfma(wf[s], xf[s], acc);
    const unsigned vq0 = r * 128 + g * 32, sq0 = Q0_OFF + kvh * Q0_G + (kq * 16 + 4 * cb + w) * 512;
    VMWAITN(8 + 2 * VEARLY);
    if (r < 3) {
      st128(rs, vq0, sq0, v4u{asu(acc[0]), tagl, asu(acc[1]), tagl});
      st128(rs, vq0 + 16, sq0, v4u{asu(acc[2]), tagl, asu(acc[3]), tagl});
    }
  }
  STAMP(4);
  if (w == 3) {
#pragma unroll
    for (int k = 0; k < 2; ++k) st128(rs, lane * 16 + k * 1024, WS_CB + c * CLR_PER_CU, v4u{SENT, SENT, SENT, SENT});
  }
#pragma unroll
  for (int k = VEARLY; k < 4; ++k) { glds(Vb, vV0 + k * 2048, LB + L_V + w * 8192 + k * 2048); glds(Vb, vV1 + k * 2048, LB + L_V + w * 8192 + k * 2048 + 1024); }
  STAMP(35);
  unsigned tw0, tw1, tw2, tw3;
  tw0 = __builtin_nontemporal_load((const GAS unsigned*)(vc + kvbase));
  tw1 = __builtin_nontemporal_load((const GAS unsigned*)(wo + (size_t)(kvh * 256 + 64 * rb) * D));
  tw2 = __builtin_nontemporal_load((const GAS unsigned*)(w1 + (size_t)(144 * w) * FF + (isF ? 32 * fi : 0)));
  tw3 = __builtin_nontemporal_load((const GAS unsigned*)(w2 + (size_t)(isF ? 32 * fi : 0) * D));
  unsigned ln = lane; OPAQUE(ln);
  const unsigned g_ = ln >> 4, r_ = ln & 15, q4_ = r_ >> 2, p4_ = r_ & 3, pc_ = ln & 7;
  const unsigned hu_ = ((r_ >> 1) & 4) * 4 + 4 * g_ + ((r_ >> 1) & 3);
  const unsigned jm = ((g_ & 1) << 1) | (g_ >> 1);
  unsigned vXg = (ln >> 2) * 128 + (ln & 3) * 16;
  unsigned aXGw = LB + L_XG + w * 576 + ln * 16;
  unsigned aXGr = LB + L_XG + w * 576 + (g_ >> 1) * 64 + ((r_ & 1) * 2 + (g_ & 1)) * 16;
  unsigned aXGr4 = g_ < 2 ? aXGr + 512 : LB + L_ZERO;
  unsigned vXPw = g_ * (4 * XP_R) + r_ * 8, vFPw = g_ * FP_R + r_ * 8;
  unsigned vXs = ln < NR ? ln * 128 : OOB;
  unsigned vRec = (r_ & 1) * 64 + g_ * 16;
  unsigned vRed = (ln >> 3) * 128 + pc_ * 16;
  unsigned vRed4 = ln < 32 ? vRed + 4096 : OOB;
  unsigned vQ = (g_ * 16 + 4 * w + q4_) * 128 + ab * 64 + p4_ * 16;
  unsigned vO = (ln >> 3) * 1024 + pc_ * 16, vML = (ln >> 3) * 128;
  unsigned vOw = ln * 4;
  unsigned vXo = (ln >> 3) * X_REP + pc_ * 8;
  unsigned vSo = (ln >> 3) * X_REP + (pc_ >> 2) * 8;
  unsigned aQIw = LB + L_QI + (64 * w + 4 * r_ + jm) * 2, aQIr = LB + L_QI + ((r_ & 1) * 128 + 8 * g_) * 2;
  unsigned aV = LB + L_V + w * 8192 + (4 * g_ + q4_) * 256 + (4 * (g_ & 1) + q4_) * 32 + 8 * p4_;
  unsigned aOWw = LB + L_OW + (w * 128 + r_) * 8;
  unsigned aMw = LB + L_ML + w * 16 + (r_ & 1) * 4;
  unsigned aOWr = LB + L_OW + (64 * (w & 1) + ln) * 8 + (w >> 1) * 4;
  unsigned aMr = LB + L_ML + (w >> 1) * 4;
  unsigned aOIw = LB + L_OI + ((w >> 1) * 64 + 32 * (w & 1) + 8 * (pc_ & 3) + 4 * (pc_ >> 2) + jm) * 2, aOIr = LB + L_OI + ((r_ & 1) * 64 + 8 * g_) * 2;
  unsigned aOA0 = (r_ & 14) == 0 ? aOIr : LB + L_ZERO, aOA1 = (r_ & 14) == 4 ? aOIr : LB + L_ZERO, aOA2 = (r_ & 14) == 8 ? aOIr : LB + L_ZERO;
  unsigned aWO = LB + L_WO + (4 * g_ + q4_) * 288 + 32 * w + 8 * p4_;
  unsigned aWO8 = LB + L_WO + (4 * g_ + q4_) * 288 + 32 * 8 + 8 * p4_;
  unsigned aRSw = LB + L_RS + (w * 32 + (jm & 1) * 16 + 2 * pc_ + (jm >> 1)) * 4, aRSr = LB + L_RS + pc_ * 16;
  unsigned aW1 = LB + L_W1 + w * 9216 + (8 * g_ + q4_) * 64 + 8 * p4_ + (g_ & 1) * 32;
  unsigned aW14 = g_ < 2 ? aW1 + 4 * 2048 : aW1;
  unsigned aW2 = LB + L_W2 + w * 9216 + (4 * g_ + q4_) * 288 + 8 * p4_;
  unsigned aRDw = LB + L_RD + (w * 3 + r_) * 128 + g_ * 16;
  unsigned aST = LB + L_ST + (r_ & 1) * 8; PIN(aST);
  unsigned aRDr = LB + L_RD + (r_ & 1) * 128 + hu_ * 4;
  unsigned v18[9], vWO[5];
#pragma unroll
  for (int k = 0; k < 9; ++k) { const unsigned n = 64 * k + ln; v18[k] = 16 * n + 864 * ((n * 3641u) >> 16); PIN(v18[k]); }
#pragma unroll
  for (int k = 0; k < 5; ++k) { const unsigned n = 64 * (w + 4 * k) + ln; vWO[k] = 16 * n + 864 * ((n * 3641u) >> 16); PIN(vWO[k]); }
  unsigned lt36 = ln < NR ? 0xFFFFFFFFu : 0u; PIN(lt36);
  PIN(vXg); PIN(aXGw); PIN(aXGr); PIN(aXGr4); PIN(vXPw); PIN(vFPw); PIN(aOA0); PIN(aOA1); PIN(aOA2); PIN(vXs); PIN(vRec); PIN(vRed); PIN(vRed4); PIN(vQ); PIN(vO); PIN(vML); PIN(vOw); PIN(vXo); PIN(vSo);
  PIN(aQIw); PIN(aQIr); PIN(aV); PIN(aOWw); PIN(aMw); PIN(aOWr); PIN(aMr); PIN(aOIw);
  PIN(aWO); PIN(aWO8); PIN(aRSw); PIN(aRSr); PIN(aW1); PIN(aW14); PIN(aW2); PIN(aRDw); PIN(aRDr);
  unsigned kvh_ = kvh, w_ = w; PINS(kvh_); PINS(w_);
  const unsigned sidx_w = (ab * 8 + asl) * 2 + (w_ >> 1);
  unsigned sOw = O0_OFF + kvh_ * O_G + sidx_w * 512 + (w_ & 1) * 256, sMLw = ML0_OFF + kvh_ * ML_G + sidx_w * 64;
  unsigned sOr = O0_OFF + kvh_ * O_G + ((w_ >> 1) * 16 + (rb >> 1)) * 512 + (rb & 1) * 256 + (w_ & 1) * 128;
  unsigned sMLr = ML0_OFF + kvh_ * ML_G + ((w_ >> 1) * 16 + (rb >> 1)) * 64;
  unsigned sXPw = XP0_OFF + 9 * cb3 * XP_R + (kvh_ * 4 + rb) * 128 + w_ * XP_R;
  unsigned sXPr = XP0_OFF + ri * XP_R + w_ * 2048;
  unsigned sX1w = X10_OFF + ri * 128, sX1r = X10_OFF + rep * X_REP + 9 * w_ * 128, sXS1r = XS10_OFF + rep * X_REP;
  unsigned sFPw = FP0_OFF + 9 * w_ * FP_R + fi * 128, sFPr = FP0_OFF + ri * FP_R + w_ * 36 * 128;
  unsigned sQ1w = Q1_OFF + kvh_ * Q_G + (kq * 16 + 4 * cb + w_) * 128, sQ1r = Q1_OFF + kvh_ * Q_G;
  unsigned sX2r = X2_OFF + rep * X_REP + 9 * kq * 128, sXS2r = XS2_OFF + rep * X_REP, sX2w = X2_OFF + ri * 128;
  PINS(sOw); PINS(sMLw); PINS(sOr); PINS(sMLr); PINS(sXPw); PINS(sXPr); PINS(sX1w); PINS(sX1r); PINS(sXS1r); PINS(sFPw); PINS(sFPr);
  PINS(sQ1w); PINS(sQ1r); PINS(sX2r); PINS(sXS2r); PINS(sX2w);
  Stats st;
  float xres[4], b2v[4], b1v;
  {
    OPAQUE(xs[0].x); OPAQUE(xs[0].y); OPAQUE(xs[0].z); OPAQUE(xs[0].w); OPAQUE(xs[1].x); OPAQUE(xs[1].y); OPAQUE(xs[1].z); OPAQUE(xs[1].w);
    OPAQUE(xs[2].x); OPAQUE(xs[2].y); OPAQUE(xs[2].z); OPAQUE(xs[2].w);
    float sa = 0.f, sq = 0.f;
#pragma unroll
    for (int k = 0; k < 3; ++k) {
      sa = dot2(xs[k].x, ONES, sa); sa = dot2(xs[k].y, ONES, sa); sa = dot2(xs[k].z, ONES, sa); sa = dot2(xs[k].w, ONES, sa);
      sq = dot2(xs[k].x, xs[k].x, sq); sq = dot2(xs[k].y, xs[k].y, sq); sq = dot2(xs[k].z, xs[k].z, sq); sq = dot2(xs[k].w, xs[k].w, sq);
    }
    float t = t32_sum(sa, sq);
    { auto s = __builtin_amdgcn_permlane16_swap(asu(t), asu(t), false, false); unsigned a = s[0], b = s[1]; t = asf(a) + asf(b); }
    t += dpp<ROR8>(t); t += dpp<ROR4>(t); t += dpp<XOR1>(t);
    st = mkstats(rdl(t, ab * 2), rdl(t, 32 + ab * 2));
    xres[0] = bflo(xr0.x); xres[1] = bfhi(xr0.x); xres[2] = bflo(xr0.y); xres[3] = bfhi(xr0.y);
    b2v[0] = bflo(b2r.x); b2v[1] = bfhi(b2r.x); b2v[2] = bflo(b2r.y); b2v[3] = bfhi(b2r.y);
    b1v = bflo(b1r);
    PIN(st.m); PIN(st.rden); PIN(b1v); PIN(xres[0]); PIN(xres[1]); PIN(xres[2]); PIN(xres[3]); PIN(b2v[0]); PIN(b2v[1]); PIN(b2v[2]); PIN(b2v[3]);
  }
  {
    STAMP(38);
    if (w == 3) {
      VMWAITN(2 * (4 - VEARLY) + 4);
      if (lane == 0) st64(rs, c * 128, WS_H0, v2u{tagl, tagh});
    }
    VMWAITN(VLEFT);
    STAMP(37);
    const unsigned vq = (g * 16 + 4 * w + q4) * 512 + p4 * 32, sq0 = Q0_OFF + kvh * Q0_G;
    v4u qa, qb, ca, cb2;
    for (unsigned sp = 0;; ++sp) {
      CFENCE();
      qa = ld128(rs, vq + ab * 128, sq0); qb = ld128(rs, vq + ab * 128 + 16, sq0);
      ca = ld128(rs, vq + 256, sq0); cb2 = ld128(rs, vq + 272, sq0);
      const bool ok = qa.y == tagl && qa.w == tagl && qb.y == tagl && qb.w == tagl &&
                      ca.y == tagl && ca.w == tagl && cb2.y == tagl && cb2.w == tagl;
      if (__all(ok) || sp > SPIN_CAP) break;
      __builtin_amdgcn_s_sleep(2);
    }
    asm volatile("" :: "v"(tw0), "v"(tw1), "v"(tw2), "v"(tw3));
    STAMP(5);
    {
      const u16* Wb = wo + (size_t)(kvh * 256 + 64 * rb) * D + 144 * cb3;
#pragma unroll
      for (int k = 0; k < 4; ++k) glds(Wb, vWO[k], LB + L_WO + (w + 4 * k) * 1024);
      if (w < 2) glds(Wb, vWO[4], LB + L_WO + (w + 16) * 1024);
      if (isF) {
        const u16* W2b = w2 + (size_t)(32 * fi) * D + 144 * w;
#pragma unroll
        for (int k = 0; k < W2EARLY; ++k) glds(W2b, v18[k], LB + L_W2 + w * 9216 + k * 1024);
        const unsigned s4 = lane & 3, chunk = (((s4 >> 1) ^ ((lane >> 5) & 1)) << 1) | (s4 & 1);
        const unsigned v1 = (144 * w + (lane >> 2)) * (FF * 2) + 64 * fi + chunk * 16;
#pragma unroll
        for (int k = 0; k < W1EARLY; ++k) glds(w1, v1 + k * 16 * (FF * 2), LB + L_W1 + w * 9216 + k * 1024);
      }
    }
    {
      unsigned a = aV; OPAQUE(a);
#pragma unroll
      for (int dt = 0; dt < 8; ++dt) vf[dt] = trfrag(a ^ (dt * 32), (a ^ (dt * 32)) + 16 * 256);
    }
    qv = v4u{qa.x, qa.z, qb.x, qb.z};
    cs = trsum4(f32x4{asf(ca.x), asf(ca.z), asf(cb2.x), asf(cb2.z)});
  }

#pragma unroll 1
  for (int lay = 0; lay < 2; ++lay) {
    const unsigned dO = lay ? O1_OFF - O0_OFF : 0u, dML = lay ? ML1_OFF - ML0_OFF : 0u, dXP = lay ? XP1_OFF - XP0_OFF : 0u;
    const unsigned dX1 = lay ? X11_OFF - X10_OFF : 0u, dFP = lay ? FP_L : 0u;
    {
      STAMP(6 + lay * 16);
      const float q = trsum4(asf4(qv));
      const unsigned qb = pk((q - st.m * cs) * st.rden, 0.f);
      *(LAS u16*)(aQIw) = (u16)qb;
    }
    BAR();
    {
      v4u qf[4];
#pragma unroll
      for (int s = 0; s < 4; ++s) qf[s] = *(LAS v4u*)(aQIr + 64 * s);
      f32x4 s0 = {0.f, 0.f, 0.f, 0.f}, s1 = {0.f, 0.f, 0.f, 0.f};
#pragma unroll
      for (int s = 0; s < 4; ++s) { s0 = mfma(kf[s], qf[s], s0); s1 = mfma(kf[4 + s], qf[s], s1); }
      float sc[8];
      {
        const float SC = 0.08838834764831845f;
        const unsigned u0 = pko(s0[0], s0[1]), u1 = pko(s0[2], s0[3]), u2 = pko(s1[0], s1[1]), u3 = pko(s1[2], s1[3]);
        const unsigned z0 = pko(bflo(u0) * SC, bfhi(u0) * SC), z1 = pko(bflo(u1) * SC, bfhi(u1) * SC);
        const unsigned z2 = pko(bflo(u2) * SC, bfhi(u2) * SC), z3 = pko(bflo(u3) * SC, bfhi(u3) * SC);
        sc[0] = bflo(z0); sc[1] = bfhi(z0); sc[2] = bflo(z1); sc[3] = bfhi(z1); sc[4] = bflo(z2); sc[5] = bfhi(z2); sc[6] = bflo(z3); sc[7] = bfhi(z3);
      }
      constexpr float EK = LOG2E;
      float mx = fmx3(fmx3(sc[0], sc[1], sc[2]), fmx3(sc[3], sc[4], sc[5]), fmx(sc[6], sc[7]));
      mx = x16_max(mx); mx = x32_max(mx);
      const float mb = -mx * EK;
      float e[8];
#pragma unroll
      for (int j = 0; j < 8; ++j) e[j] = ex2(__builtin_fmaf(sc[j], EK, mb));
      v4u pb; pb.x = pk(e[0], e[1]); pb.y = pk(e[2], e[3]); pb.z = pk(e[4], e[5]); pb.w = pk(e[6], e[7]);
      f32x4 oa[8];
#pragma unroll
      for (int dt = 0; dt < 8; ++dt) oa[dt] = mfma(pb, vf[dt], f32x4{0.f, 0.f, 0.f, 0.f});
      const f32x4 la = mfma(pb, v4u{ONES, ONES, ONES, ONES}, f32x4{0.f, 0.f, 0.f, 0.f});
#pragma unroll
      for (int dt = 0; dt < 8; ++dt) KEEP4(oa[dt]);
      KEEP4(la);
      if (g == 0) {
#pragma unroll
        for (int dt = 0; dt < 8; ++dt) { v2u t; t.x = asu(oa[dt][0]); t.y = asu(oa[dt][1]); *(LAS v2u*)(aOWw + dt * 128) = t; }
        if (r < 2) { *(LAS float*)(aMw) = mb; *(LAS float*)(aMw + 8) = r ? la[1] : la[0]; }
      }
    }
    BAR();
    {
      float m[4], l[4], o[4];
#pragma unroll
      for (int k = 0; k < 4; ++k) {
        m[k] = *(LAS float*)(aMr + k * 16);
        l[k] = *(LAS float*)(aMr + k * 16 + 8);
        o[k] = *(LAS float*)(aOWr + k * 1024);
      }
      const float M = __builtin_elementwise_minimum(__builtin_elementwise_minimum(m[0], m[1]), __builtin_elementwise_minimum(m[2], m[3]));
      float f[4];
#pragma unroll
      for (int k = 0; k < 4; ++k) f[k] = ex2(M - m[k]);
      const float os = __builtin_fmaf(f[0], o[0], f[1] * o[1]) + __builtin_fmaf(f[2], o[2], f[3] * o[3]);
      const float lsum = __builtin_fmaf(f[0], l[0], f[1] * l[1]) + __builtin_fmaf(f[2], l[2], f[3] * l[3]);
      st32(rs, vOw, (sOw + dO), asu(os));
      if ((w & 1) == 0 && lane == 0) st64(rs, 0, (sMLw + dML), v2u{asu(M), asu(lsum)});
    }
    STAMP(7 + lay * 16);
    if (lay == 0) {
      const v4u ones = {SENT, SENT, SENT, SENT};
      unsigned a0 = sQ1w, a1 = sOw, a2 = sMLw, a3 = sXPw, a4 = sFPw, a5 = sX1w;
      PINS(a0); PINS(a1); PINS(a2); PINS(a3); PINS(a4); PINS(a5);
      st32(rs, vOw, a1 + (O1_OFF - O0_OFF), SENT);
      if ((w & 1) == 0 && lane == 0) st64(rs, 0, a2 + (ML1_OFF - ML0_OFF), v2u{SENT, SENT});
      const v2u ones2 = {SENT, SENT};
      if (r < 2) st128(rs, vRec, a0, ones);
      if (g < (w == 0 ? 3u : 2u)) st64(rs, vXPw, a3 + (XP1_OFF - XP0_OFF), ones2);
      if (isF) {
        st64(rs, vFPw, a4, ones2); st64(rs, vFPw, a4 + 4 * FP_R, ones2);
        st64(rs, vFPw, a4 + FP_L, ones2); st64(rs, vFPw, a4 + FP_L + 4 * FP_R, ones2);
        if (g == 0) { st64(rs, vFPw, a4 + 8 * FP_R, ones2); st64(rs, vFPw, a4 + FP_L + 8 * FP_R, ones2); }
      }
      if (isR && w == 0) {
        st64(rs, vXo, a5 + (X11_OFF - X10_OFF), v2u{SENT, SENT});
        if ((pc & 3) == 0) st64(rs, vSo, a5 + (XS11_OFF - X10_OFF), v2u{SENT, SENT});
      }
    }
    {
      v4u ov; v2u ml;
      delay<DLY_H2>();
      if (lay == 0) {
        for (unsigned sp = 0; sp <= SPIN_CAP; ++sp) {
          CFENCE();
          const v2u t0 = ld64(rs, tid * 128, WS_H0);
          ov = ld128(rs, vO, sOr); ml = ld64(rs, vML, sMLr);
          if (__all(t0.x == tagl && t0.y == tagh && um3(vmax1(ov), ml.x, ml.y) != SENT)) break;
          __builtin_amdgcn_s_sleep(PSLEEP);
        }
      } else {
        POLL(ov = ld128(rs, vO, (sOr + dO)); ml = ld64(rs, vML, (sMLr + dML)), um3(vmax1(ov), ml.x, ml.y));
      }
      STAMP(8 + lay * 16);
      const float ms = asf(ml.x);
      float M = __builtin_elementwise_minimum(ms, dpp<ROR8>(ms));
      { auto s = __builtin_amdgcn_permlane16_swap(asu(M), asu(M), false, false); unsigned x = s[0], y = s[1]; M = __builtin_elementwise_minimum(asf(x), asf(y)); }
      { auto s = __builtin_amdgcn_permlane32_swap(asu(M), asu(M), false, false); unsigned x = s[0], y = s[1]; M = __builtin_elementwise_minimum(asf(x), asf(y)); }
      const float f = ex2(M - ms);
      float lsum = f * asf(ml.y);
      f32x4 o = asf4(ov) * f;
      lsum += dpp<ROR8>(lsum);
      float t = trsum4(o);
      { auto s = __builtin_amdgcn_permlane16_swap(asu(lsum), asu(lsum), false, false); unsigned x = s[0], y = s[1]; lsum = asf(x) + asf(y); }
      t += dpp<ROR8>(t);
      { auto s = __builtin_amdgcn_permlane32_swap(asu(lsum), asu(lsum), false, false); unsigned x = s[0], y = s[1]; lsum = asf(x) + asf(y); }
      const unsigned ob = pk(t * rcp(lsum), 0.f);
      *(LAS u16*)(aOIw) = (u16)ob;
      if (lay == 0) VMWAIT(0);
      BAR();
      const v4u oa00 = *(LAS v4u*)(aOA0), oa01 = *(LAS v4u*)(aOA0 + 64), oa10 = *(LAS v4u*)(aOA1), oa11 = *(LAS v4u*)(aOA1 + 64);
      const v4u wf0 = trfrag(aWO, aWO + 16 * 288), wf1 = trfrag(aWO + 32 * 288, aWO + 48 * 288);
      const v4u wf2 = trfrag(aWO + 128, aWO + 128 + 16 * 288), wf3 = trfrag(aWO + 128 + 32 * 288, aWO + 128 + 48 * 288);
      f32x4 acc = mfma(oa00, wf0, f32x4{0.f, 0.f, 0.f, 0.f}), acc2 = mfma(oa10, wf2, f32x4{0.f, 0.f, 0.f, 0.f});
      if (w == 0) {
        const v4u oa20 = *(LAS v4u*)(aOA2), oa21 = *(LAS v4u*)(aOA2 + 64);
        const v4u wf4 = trfrag(aWO8, aWO8 + 16 * 288), wf5 = trfrag(aWO8 + 32 * 288, aWO8 + 48 * 288);
        acc = mfma(oa20, wf4, acc); acc2 = mfma(oa21, wf5, acc2);
      }
      acc = mfma(oa01, wf1, acc); acc2 = mfma(oa11, wf3, acc2);
      if (g < (w == 0 ? 3u : 2u)) st64(rs, vXPw, (sXPw + dXP), v2u{asu(acc[0] + acc2[0]), asu(acc[1] + acc2[1])});
    }
    STAMP(9 + lay * 16);
    if (lay == 0 && isF) {
      unsigned ln2 = lane, wo_ = w, fo_ = fi; OPAQUE(ln2); PINS(wo_); PINS(fo_);
      const unsigned s4 = ln2 & 3, chunk = (((s4 >> 1) ^ ((ln2 >> 5) & 1)) << 1) | (s4 & 1);
      const unsigned v1 = (144 * wo_ + (ln2 >> 2)) * (FF * 2) + 64 * fo_ + chunk * 16;
#pragma unroll
      for (int k = W1EARLY; k < 9; ++k) glds(w1, v1 + k * 16 * (FF * 2), LB + L_W1 + wo_ * 9216 + k * 1024);
      const u16* W2b = w2 + (size_t)(32 * fo_) * D + 144 * wo_;
#pragma unroll
      for (int k = W2EARLY; k < 9; ++k) glds(W2b, v18[k], LB + L_W2 + wo_ * 9216 + k * 1024);
    }
    if (isR) {
      v4u pv0, pv1;
      delay<DLY_H3>();
      POLL(pv0 = ld128(rs, vRed, (sXPr + dXP)); pv1 = ld128(rs, vRed + 1024, (sXPr + dXP)), vmax2(pv0, pv1));
      STAMP(10 + lay * 16);
      float t = trsum4(asf4(pv0) + asf4(pv1));
      t += dpp<ROR8>(t);
      *(LAS float*)(aRSw) = t;
      BAR();
      if (w == 0) {
        const f32x4 s0 = *(LAS f32x4*)(aRSr), s1 = *(LAS f32x4*)(aRSr + 128), s2 = *(LAS f32x4*)(aRSr + 256), s3 = *(LAS f32x4*)(aRSr + 384);
        const f32x4 s = (s0 + s1) + (s2 + s3);
        const unsigned y0 = pko(s[0], s[1]), y1 = pko(s[2], s[3]);
        v2u xo; xo.x = san(pko(xres[0] + bflo(y0), xres[1] + bfhi(y0))); xo.y = san(pko(xres[2] + bflo(y1), xres[3] + bfhi(y1)));
        xres[0] = bflo(xo.x); xres[1] = bfhi(xo.x); xres[2] = bflo(xo.y); xres[3] = bfhi(xo.y);
        float sa = (xres[0] + xres[1]) + (xres[2] + xres[3]);
        float sq = __builtin_fmaf(xres[0], xres[0], xres[1] * xres[1]) + __builtin_fmaf(xres[2], xres[2], xres[3] * xres[3]);
        sa += dpp<XOR1>(sa); sq += dpp<XOR1>(sq); sa += dpp<XOR2>(sa); sq += dpp<XOR2>(sq);
        if ((pc & 3) == 0) st64(rs, vSo, (sX1w + dX1) + X1_L, v2u{asu(sa), asu(sq)});
        CFENCE();
        st64(rs, vXo, (sX1w + dX1), xo);
      }
      STAMP(11 + lay * 16);
    }
    if (isF) {
      v4u a0[5], a1[5];
      delay<DLY_H4>();
      if (lay == 0) VMWAIT(0);
#pragma unroll
      for (int s = 0; s < 4; ++s) {
        a0[s] = trfrag(aW1 + s * 2048, aW1 + s * 2048 + 256);
        a1[s] = trfrag((aW1 ^ 32) + s * 2048, (aW1 ^ 32) + s * 2048 + 256);
      }
      a0[4] = trfrag(aW14, aW14 + 256); a1[4] = trfrag(aW14 ^ 32, (aW14 ^ 32) + 256);
      v4u x1[5], s1v, xg0;
      asm volatile("" : "=v"(xg0));
      if (w == 3) {
        POLLM(
          s1v = ld128(rs, vXs, (sXS1r + dX1));
          if (lane < NR) xg0 = ld128(rs, vXg, (sX1r + dX1)),
          um2(vmax1(xg0) & lt36, vmax1(s1v)) == SENT);
      } else {
        POLLM(if (lane < NR) xg0 = ld128(rs, vXg, (sX1r + dX1)), (vmax1(xg0) & lt36) == SENT);
      }
      if (lane < NR) *(LAS v4u*)(aXGw) = xg0;
      asm volatile("s_waitcnt lgkmcnt(0)" ::: "memory");
#pragma unroll
      for (int s = 0; s < 4; ++s) x1[s] = *(LAS v4u*)(aXGr + s * 128);
      x1[4] = *(LAS v4u*)(aXGr4);
      if (lay == 0 && r == 2) {
#pragma unroll
        for (int s = 0; s < 4; ++s) x1[s] = v4u{ONES, ONES, ONES, ONES};
        if (g < 2) x1[4] = v4u{ONES, ONES, ONES, ONES};
      }
      STAMP(12 + lay * 16);
      f32x4 h0 = {0.f, 0.f, 0.f, 0.f}, h1 = {0.f, 0.f, 0.f, 0.f};
#pragma unroll
      for (int s = 0; s < 5; ++s) { h0 = mfma(a0[s], x1[s], h0); h1 = mfma(a1[s], x1[s], h1); }
      if (w == 3) {
        const float t = stats_reduce(s1v);
        auto s = __builtin_amdgcn_permlane32_swap(asu(t), asu(t), false, false);
        const unsigned x = s[0], y = s[1];
        const Stats z = mkstats(asf(x), asf(y));
        if (r == 0 && g < 2) *(LAS v2u*)(LB + L_ST + g * 8) = v2u{asu(z.m), asu(z.rden)};
      }
      if (r < 3) { *(LAS f32x4*)(aRDw) = h0; *(LAS f32x4*)(aRDw + 64) = h1; }
      BAR();
      const v2u smr = *(LAS v2u*)(aST);
      const float sm = asf(smr.x), sr = asf(smr.y);
      const float p0 = *(LAS float*)(aRDr), p1 = *(LAS float*)(aRDr + 384), p2 = *(LAS float*)(aRDr + 768), p3 = *(LAS float*)(aRDr + 1152);
      v4u w2f[9];
#pragma unroll
      for (int t = 0; t < 9; ++t) w2f[t] = trfrag(aW2 + 32 * t, aW2 + 32 * t + 16 * 288);
      const float hs = (p0 + p1) + (p2 + p3);
      if (lay == 0) {
        unsigned ac = aRDr; OPAQUE(ac); ac += (2 - (r & 1)) * 128;
        cs1 = (*(LAS float*)(ac) + *(LAS float*)(ac + 384)) + (*(LAS float*)(ac + 768) + *(LAS float*)(ac + 1152));
      }
      const float pre = rbf((hs - sm * cs1) * sr);
      const float a = rbf(pre + b1v);
      const float u = a * __builtin_fmaf(a * a, -2.0f * LOG2E * 0.7978845608028654f * 0.044715f, -2.0f * LOG2E * 0.7978845608028654f);
      const float hv = a * rcp(1.0f + ex2(u));
      const unsigned hp = pk(hv, dpp<SHL2>(hv));
#define HAV(G, C, CTRL) hA[G].C = (unsigned)__builtin_amdgcn_update_dpp((int)hA[G].C, (int)hp, CTRL, 0xf, 1 << G, true)
      HAV(0, x, 0xE4); HAV(0, y, 0x104); HAV(0, z, 0x108); HAV(0, w, 0x10C);
      HAV(1, x, 0x114); HAV(1, y, 0xE4); HAV(1, z, 0x104); HAV(1, w, 0x108);
      HAV(2, x, 0x118); HAV(2, y, 0x114); HAV(2, z, 0xE4); HAV(2, w, 0x104);
      HAV(3, x, 0x11C); HAV(3, y, 0x118); HAV(3, z, 0x114); HAV(3, w, 0xE4);
#undef HAV
      f32x4 f0 = mfma(hA[0], w2f[0], f32x4{0.f, 0.f, 0.f, 0.f}), f1 = mfma(hA[0], w2f[4], f32x4{0.f, 0.f, 0.f, 0.f});
      const f32x4 f2 = mfma(hA[0], w2f[8], f32x4{0.f, 0.f, 0.f, 0.f});
#pragma unroll
      for (int t = 1; t < 4; ++t) { f0 = mfma(hA[t], w2f[t], f0); f1 = mfma(hA[t], w2f[4 + t], f1); }
      st64(rs, vFPw, (sFPw + dFP), v2u{asu(f0[0]), asu(f0[1])});
      st64(rs, vFPw, (sFPw + dFP) + 4 * FP_R, v2u{asu(f1[0]), asu(f1[1])});
      if (g == 0) st64(rs, vFPw, (sFPw + dFP) + 8 * FP_R, v2u{asu(f2[0]), asu(f2[1])});
      STAMP(13 + lay * 16);
    }
    if (isR) {
      v4u pv[5];
      delay<DLY_H5>();
      POLL(
        for (int k = 0; k < 4; ++k) pv[k] = ld128(rs, vRed + k * 1024, (sFPr + dFP));
        pv[4] = ld128(rs, vRed4, (sFPr + dFP)),
        vmax5(pv));
      STAMP(14 + lay * 16);
      float t = trsum4(((asf4(pv[0]) + asf4(pv[1])) + (asf4(pv[2]) + asf4(pv[3]))) + asf4(pv[4]));
      t += dpp<ROR8>(t);
      *(LAS float*)(aRSw) = t;
      BAR();
      if (w == 0) {
        const f32x4 s0 = *(LAS f32x4*)(aRSr), s1 = *(LAS f32x4*)(aRSr + 128), s2 = *(LAS f32x4*)(aRSr + 256), s3 = *(LAS f32x4*)(aRSr + 384);
        const f32x4 s = (s0 + s1) + (s2 + s3);
        const unsigned y0 = pko(s[0], s[1]), y1 = pko(s[2], s[3]);
        const unsigned z0 = pko(bflo(y0) + b2v[0], bfhi(y0) + b2v[1]), z1 = pko(bflo(y1) + b2v[2], bfhi(y1) + b2v[3]);
        v2u xo; xo.x = pko(xres[0] + bflo(z0), xres[1] + bfhi(z0)); xo.y = pko(xres[2] + bflo(z1), xres[3] + bfhi(z1));
        if (lay == 0) {
          xo.x = san(xo.x); xo.y = san(xo.y);
          st64(rs, vXo, sX2w, xo);
          st64(rs, vXo, sX2w + 8 * X_REP, xo);
          xres[0] = bflo(xo.x); xres[1] = bfhi(xo.x); xres[2] = bflo(xo.y); xres[3] = bfhi(xo.y);
          float sa = (xres[0] + xres[1]) + (xres[2] + xres[3]);
          float sq = __builtin_fmaf(xres[0], xres[0], xres[1] * xres[1]) + __builtin_fmaf(xres[2], xres[2], xres[3] * xres[3]);
          sa += dpp<XOR1>(sa); sq += dpp<XOR1>(sq); sa += dpp<XOR2>(sa); sq += dpp<XOR2>(sq);
          if ((pc & 3) == 0) {
            st64(rs, vSo, sX2w + 16 * X_REP, v2u{asu(sa), asu(sq)});
            st64(rs, vSo, sX2w + 24 * X_REP, v2u{asu(sa), asu(sq)});
          }
        } else {
          unsigned ln2 = lane, ro_ = ri; OPAQUE(ln2); PINS(ro_);
          if (ln2 < 8) *(GAS v2u*)(out + (ln2 >> 2) * D + 16 * ro_ + 4 * (ln2 & 3)) = xo;
        }
      }
      STAMP(15 + lay * 16);
    }
    if (lay == 0) {
      v4u wf[5], xg[5], sv;
#pragma unroll
      for (int s = 0; s < 4; ++s) wf[s] = trfrag(aWQ + s * 4096, aWQ + s * 4096 + 512);
      wf[4] = trfrag(aWQ4, aWQ4 + 512);
      if (isR) delay<DLY_H6R>(); else if (isF) delay<DLY_H6F>(); else delay<DLY_H6I>();
      v4u xg0;
      asm volatile("" : "=v"(xg0));
      POLLM(if (lane < NR) xg0 = ld128(rs, vXg, sX2r), (vmax1(xg0) & lt36) == SENT);
      if (lane < NR) *(LAS v4u*)(aXGw) = xg0;
      sv = ld128(rs, vXs, sXS2r);
      asm volatile("s_waitcnt lgkmcnt(0)" ::: "memory");
#pragma unroll
      for (int s = 0; s < 4; ++s) xg[s] = *(LAS v4u*)(aXGr + s * 128);
      xg[4] = *(LAS v4u*)(aXGr4);
      STAMP(19);
      f32x4 acc = {0.f, 0.f, 0.f, 0.f};
#pragma unroll
      for (int s = 0; s < 5; ++s) acc = mfma(wf[s], xg[s], acc);
      if (r < 2) st128(rs, vRec, sQ1w, asu4(acc));
      STAMP(20);
      {
        unsigned a = aV; OPAQUE(a);
#pragma unroll
        for (int dt = 0; dt < 8; ++dt) vf[dt] = trfrag(a ^ (dt * 32), (a ^ (dt * 32)) + 16 * 256);
      }
      { unsigned sp_ = 0; while (__builtin_expect(__builtin_amdgcn_ballot_w64(vmax1(sv) == SENT) != 0ull, 0) && ++sp_ <= SPIN_CAP) { CFENCE(); sv = ld128(rs, vXs, sXS2r); } }
      const float t = stats_reduce(sv);
      st = mkstats(rdl(t, 16 * ab), rdl(t, 32 + 16 * ab));
      PIN(st.m); PIN(st.rden);
      STAMP(21);
      delay<DLY_H1>();
      POLL(qv = ld128(rs, vQ, sQ1r), vmax1(qv));
    }
  }
  if (c == 0 && tid == 0) st64(rs, 0, WS_E, v2u{tagl, tagh});
}

}

extern "C" void kernel_launch(void* const* d_in, const int* in_sizes, int n_in, void* d_out, int out_size, void* d_ws, size_t ws_size,
                              hipStream_t stream) {
  (void)in_sizes; (void)n_in; (void)out_size; (void)ws_size;
  (void)hipFuncSetAttribute((const void*)fk::fused_decode, hipFuncAttributeMaxDynamicSharedMemorySize, (int)fk::L_END);
  fk::fused_decode<<<256, 256, fk::L_END, stream>>>((const u16*)d_in[0], (const u16*)d_in[1], (const u16*)d_in[2], (const u16*)d_in[3],
                                                    (const u16*)d_in[4], (const u16*)d_in[6], (const u16*)d_in[7], (const u16*)d_in[8],
                                                    (const u16*)d_in[9], (u16*)d_out, (unsigned char*)d_ws);
}
```

```cpp
#include <hip/hip_runtime.h>
#include <stdint.h>

typedef unsigned short u16;
typedef unsigned v4u __attribute__((ext_vector_type(4)));
typedef unsigned v2u __attribute__((ext_vector_type(2)));
typedef float f32x4 __attribute__((ext_vector_type(4)));
typedef __bf16 bf16x8 __attribute__((ext_vector_type(8)));
typedef __bf16 bf16x2 __attribute__((ext_vector_type(2)));
typedef short s16x4 __attribute__((ext_vector_type(4)));
#define LAS __attribute__((address_space(3)))
#define GAS __attribute__((address_space(1)))

#define STAMPS 0

#pragma clang diagnostic ignored "-Wint-to-pointer-cast"
namespace fk {

constexpr int D = 576, FF = 4608, SK = 4096, WIN = 1024, DH = 128;
constexpr int NR = 36;

constexpr unsigned WS_E = 0, WS_H0 = 4096;
constexpr unsigned Q0_OFF = WS_H0 + 256 * 128, Q0_G = 64 * 512;
constexpr unsigned O_G = 16384, O_L = 16 * O_G;
constexpr unsigned ML_G = 2048, ML_L = 16 * ML_G;
constexpr unsigned XP_R = 64 * 128, XP_L = NR * XP_R;
constexpr unsigned X_REP = NR * 128, X1_L = 8 * X_REP;
constexpr unsigned WS_CB = Q0_OFF + 16 * Q0_G;
constexpr unsigned XP0_OFF = WS_CB;
constexpr unsigned X10_OFF = XP0_OFF + XP_L, XS10_OFF = X10_OFF + X1_L;
constexpr unsigned X2_OFF = XS10_OFF + X1_L, XS2_OFF = X2_OFF + 16 * X_REP;
constexpr unsigned CB_END = XS2_OFF + 16 * X_REP, CLR_PER_CU = 2048;
static_assert(CB_END <= WS_CB + 256 * CLR_PER_CU, "clear region");
constexpr unsigned Q_G = 8192, Q_L = 16 * Q_G;
constexpr unsigned FP_R = 144 * 128, FP_L = NR * FP_R;
constexpr unsigned O0_OFF = WS_CB + 256 * CLR_PER_CU, ML0_OFF = O0_OFF + O_L;
constexpr unsigned Q1_OFF = ML0_OFF + ML_L;
constexpr unsigned O1_OFF = Q1_OFF + Q_L, ML1_OFF = O1_OFF + O_L, XP1_OFF = ML1_OFF + ML_L;
constexpr unsigned X11_OFF = XP1_OFF + XP_L, XS11_OFF = X11_OFF + X1_L;
constexpr unsigned FP0_OFF = XS11_OFF + X1_L, FP1_OFF = FP0_OFF + FP_L;
constexpr unsigned WS_USED = FP1_OFF + FP_L;
constexpr unsigned WS_DBG = WS_USED;
constexpr unsigned OOB = 0x80000000u;

constexpr unsigned L_WQ = 0, L_V = 18432, L_WO = L_V + 32768, L_W1 = L_WO + 18432, L_W2 = L_W1 + 36864;
constexpr unsigned L_QI = L_W2 + 36864, L_OW = L_QI + 512, L_ML = L_OW + 4096, L_OI = L_ML + 64, L_RD = L_OI + 256;
constexpr unsigned L_RS = L_RD + 4 * 3 * 128, L_XG = L_RS + 512, L_ZERO = L_XG + 4 * 576, L_ST = L_ZERO + 128, L_END = L_ST + 16;
static_assert(L_END <= 163840, "LDS");

constexpr unsigned SENT = 0xFFFFFFFFu;
constexpr unsigned SPIN_CAP = 1u << 16;
constexpr int AUX_SC1 = 16, AUX_NT = 2;
constexpr unsigned ONES = 0x3F803F80u;

__device__ __forceinline__ float asf(unsigned u) { return __builtin_bit_cast(float, u); }
__device__ __forceinline__ unsigned asu(float f) { return __builtin_bit_cast(unsigned, f); }
__device__ __forceinline__ bf16x2 asbf2(unsigned u) { return __builtin_bit_cast(bf16x2, u); }
__device__ __forceinline__ unsigned pk(float lo, float hi) { bf16x2 v = {(__bf16)lo, (__bf16)hi}; return __builtin_bit_cast(unsigned, v); }
__device__ __forceinline__ float bflo(unsigned u) { return asf(u << 16); }
__device__ __forceinline__ float bfhi(unsigned u) { return asf(u & 0xFFFF0000u); }
__device__ __forceinline__ float rbf(float x) { return bflo(pk(x, x)); }
__device__ __forceinline__ unsigned san(unsigned u) { return u < 0xFFFFFFFEu ? u : 0xFFFFFFFEu; }
__device__ __forceinline__ f32x4 asf4(v4u v) { f32x4 o; o.x = asf(v.x); o.y = asf(v.y); o.z = asf(v.z); o.w = asf(v.w); return o; }
__device__ __forceinline__ v4u asu4(f32x4 v) { v4u o; o.x = asu(v.x); o.y = asu(v.y); o.z = asu(v.z); o.w = asu(v.w); return o; }
__device__ __forceinline__ unsigned um2(unsigned a, unsigned b) { return a > b ? a : b; }
__device__ __forceinline__ unsigned um3(unsigned a, unsigned b, unsigned c) { return um2(um2(a, b), c); }
__device__ __forceinline__ unsigned vmax1(v4u a) { return um2(um3(a.x, a.y, a.z), a.w); }
__device__ __forceinline__ unsigned vmax2(v4u a, v4u b) { return um3(um3(a.x, a.y, a.z), um3(a.w, b.x, b.y), um2(b.z, b.w)); }
__device__ __forceinline__ unsigned vmax5(const v4u (&a)[5]) {
  return um3(um3(um3(a[0].x, a[0].y, a[0].z), um3(a[0].w, a[1].x, a[1].y), um3(a[1].z, a[1].w, a[2].x)),
             um3(um3(a[2].y, a[2].z, a[2].w), um3(a[3].x, a[3].y, a[3].z), um3(a[3].w, a[4].x, a[4].y)), um2(a[4].z, a[4].w));
}
__device__ __forceinline__ f32x4 mfma(v4u a, v4u b, f32x4 c) {
  return __builtin_amdgcn_mfma_f32_16x16x32_bf16(__builtin_bit_cast(bf16x8, a), __builtin_bit_cast(bf16x8, b), c, 0, 0, 0);
}
__device__ __forceinline__ float dot2(unsigned a, unsigned b, float c) { return __builtin_amdgcn_fdot2_f32_bf16(asbf2(a), asbf2(b), c, false); }
template <int CTRL> __device__ __forceinline__ unsigned dppu(unsigned x) { return (unsigned)__builtin_amdgcn_mov_dpp((int)x, CTRL, 0xf, 0xf, true); }
template <int CTRL> __device__ __forceinline__ float dpp(float x) { return asf(dppu<CTRL>(asu(x))); }
constexpr int XOR1 = 0xB1, XOR2 = 0x4E, ROR4 = 0x124, ROR8 = 0x128, SHL2 = 0x102, SHL4 = 0x104, SHL8 = 0x108, SHL12 = 0x10C;
__device__ __forceinline__ float t32_sum(float a, float b) { auto s = __builtin_amdgcn_permlane32_swap(asu(a), asu(b), false, false); unsigned x = s[0], y = s[1]; return asf(x) + asf(y); }
__device__ __forceinline__ float t16_sum(float a, float b) { auto s = __builtin_amdgcn_permlane16_swap(asu(a), asu(b), false, false); unsigned x = s[0], y = s[1]; return asf(x) + asf(y); }
__device__ __forceinline__ float trsum4(f32x4 v) { return t16_sum(t32_sum(v.x, v.y), t32_sum(v.z, v.w)); }
__device__ __forceinline__ float fmx(float a, float b) { return __builtin_elementwise_maximum(a, b); }
__device__ __forceinline__ float fmx3(float a, float b, float c) { return fmx(fmx(a, b), c); }
__device__ __forceinline__ float x16_max(float x) { auto s = __builtin_amdgcn_permlane16_swap(asu(x), asu(x), false, false); unsigned a = s[0], b = s[1]; return fmx(asf(a), asf(b)); }
__device__ __forceinline__ float x32_max(float x) { auto s = __builtin_amdgcn_permlane32_swap(asu(x), asu(x), false, false); unsigned a = s[0], b = s[1]; return fmx(asf(a), asf(b)); }
__device__ __forceinline__ unsigned pko(float lo, float hi) { unsigned u = pk(lo, hi); asm("" : "+v"(u)); return u; }
#define KEEP4(x) asm volatile("" :: "v"(x))
__device__ __forceinline__ float ex2(float x) { return __builtin_amdgcn_exp2f(x); }
__device__ __forceinline__ float rcp(float x) { return __builtin_amdgcn_rcpf(x); }
__device__ __forceinline__ float rdl(float x, int l) { return asf((unsigned)__builtin_amdgcn_readlane((int)asu(x), l)); }
constexpr float LOG2E = 1.4426950408889634f;

#define BAR() asm volatile("s_waitcnt lgkmcnt(0)\n\ts_barrier" ::: "memory")
#define VMWAIT(n) asm volatile("s_waitcnt vmcnt(" #n ")" ::: "memory")
#define VMWAITN(n) asm volatile("s_waitcnt vmcnt(%0)" :: "n"(n) : "memory")
#define CFENCE() asm volatile("" ::: "memory")
#define OPAQUE(x) asm volatile("" : "+v"(x))
#define PIN(x) asm volatile("" : "+v"(x))
#define PINS(x) asm volatile("" : "+s"(x))
#define SBAR() __builtin_amdgcn_sched_barrier(0)

__device__ __forceinline__ void glds(const void* base, unsigned voff, unsigned ldsd) {
  unsigned keep;
  asm volatile("s_mov_b32 %0, m0\n\ts_mov_b32 m0, %3\n\ts_nop 0\n\tglobal_load_lds_dwordx4 %1, %2 nt\n\ts_mov_b32 m0, %0"
               : "=&s"(keep) : "v"(voff), "s"(base), "s"(ldsd) : "memory");
}
__device__ __forceinline__ v4u trfrag(unsigned a0, unsigned a1) {
  s16x4 x = __builtin_amdgcn_ds_read_tr16_b64_v4i16((LAS s16x4*)(a0));
  s16x4 y = __builtin_amdgcn_ds_read_tr16_b64_v4i16((LAS s16x4*)(a1));
  v2u ux = __builtin_bit_cast(v2u, x), uy = __builtin_bit_cast(v2u, y);
  v4u o; o.x = ux.x; o.y = ux.y; o.z = uy.x; o.w = uy.y; return o;
}
typedef __amdgpu_buffer_rsrc_t rsrc_t;
__device__ __forceinline__ v4u ld128(rsrc_t rs, unsigned v, unsigned s) { return __builtin_amdgcn_raw_buffer_load_b128(rs, (int)v, (int)s, AUX_SC1); }
__device__ __forceinline__ v2u ld64(rsrc_t rs, unsigned v, unsigned s) { return __builtin_amdgcn_raw_buffer_load_b64(rs, (int)v, (int)s, AUX_SC1); }
__device__ __forceinline__ void st128(rsrc_t rs, unsigned v, unsigned s, v4u d) { __builtin_amdgcn_raw_buffer_store_b128(d, rs, (int)v, (int)s, AUX_SC1); }
__device__ __forceinline__ void st64(rsrc_t rs, unsigned v, unsigned s, v2u d) { __builtin_amdgcn_raw_buffer_store_b64(d, rs, (int)v, (int)s, AUX_SC1); }
__device__ __forceinline__ void st32(rsrc_t rs, unsigned v, unsigned s, unsigned d) { __builtin_amdgcn_raw_buffer_store_b32(d, rs, (int)v, (int)s, AUX_SC1); }

__device__ __forceinline__ v4u ald128(rsrc_t rs, unsigned v) { v4u d; asm volatile("buffer_load_dwordx4 %0, %1, %2, 0 offen" : "=v"(d) : "v"(v), "s"(rs) : "memory"); return d; }
__device__ __forceinline__ v2u ald64(rsrc_t rs, unsigned v) { v2u d; asm volatile("buffer_load_dwordx2 %0, %1, %2, 0 offen" : "=v"(d) : "v"(v), "s"(rs) : "memory"); return d; }
__device__ __forceinline__ v2u ald64c(rsrc_t rs, unsigned v) { v2u d; asm volatile("buffer_load_dwordx2 %0, %1, %2, 0 offen sc1" : "=v"(d) : "v"(v), "s"(rs) : "memory"); return d; }
__device__ __forceinline__ unsigned ald16(rsrc_t rs, unsigned v) { unsigned d; asm volatile("buffer_load_ushort %0, %1, %2, 0 offen" : "=v"(d) : "v"(v), "s"(rs) : "memory"); return d; }

__device__ __forceinline__ v4u ald128c(rsrc_t rs, unsigned v, unsigned so) { v4u d; asm volatile("buffer_load_dwordx4 %0, %1, %2, %3 offen sc1" : "=v"(d) : "v"(v), "s"(rs), "s"(so) : "memory"); return d; }

__device__ __forceinline__ unsigned aldw(const void* base) { unsigned d; const unsigned z = 0; asm volatile("global_load_dword %0, %1, %2 nt" : "=v"(d) : "v"(z), "s"(base) : "memory"); return d; }

struct Stats { float m, rden; };
__device__ __forceinline__ Stats mkstats(float sx, float sxx) {
  const float mean = sx * (1.0f / D);
  float var = __builtin_fmaf(-mean, mean, sxx * (1.0f / D));
  var = var > 0.f ? var : 0.f;
  const float den = rbf(__builtin_amdgcn_sqrtf(rbf(rbf(var) + 1e-5f)));
  Stats s; s.m = rbf(mean); s.rden = rcp(den); return s;
}
__device__ __forceinline__ float stats_reduce(v4u v) {
  float t = trsum4(asf4(v));
  t += dpp<ROR8>(t); t += dpp<ROR4>(t); t += dpp<XOR2>(t); t += dpp<XOR1>(t);
  return t;
}

#define STAMP(k) do {} while (0)
#define MIMIC 0
#define DLY_H1 2
#define DLY_H2 12
#define DLY_H3 12
#define DLY_H4 40
#define DLY_H5 44
#define DLY_H6R 14
#define DLY_H6F 42
#define DLY_H6I 90
#define PSLEEP 1
#define H1MODE 0
#define GAP_H1 6
#define W1EARLY 0
#define W2POS 1
#define VMID 0
#define VEARLY 2
#define VLEFT 12
#define W2EARLY 6
template <int N> __device__ __forceinline__ void delay() { if constexpr (N > 0) __builtin_amdgcn_s_sleep(N); }

#define POLL(LOADS, MAXV) do { unsigned sp_ = 0; \
    do { CFENCE(); LOADS; if (__builtin_expect(__builtin_amdgcn_ballot_w64((MAXV) == SENT) == 0ull, 1)) break; __builtin_amdgcn_s_sleep(PSLEEP); } while (++sp_ <= SPIN_CAP); } while (0)

#define POLL2(T, RES, LOAD, BAD, GAP) do { T pa_, pb_; unsigned sp_ = 0; \
    pa_ = LOAD; __builtin_amdgcn_s_sleep(GAP); CFENCE(); pb_ = LOAD; \
    for (;;) { \
      { const T x_ = pa_; if (__builtin_amdgcn_ballot_w64(BAD) == 0ull) { RES = pa_; break; } } \
      CFENCE(); pa_ = LOAD; \
      { const T x_ = pb_; if (__builtin_amdgcn_ballot_w64(BAD) == 0ull || ++sp_ > SPIN_CAP) { RES = pb_; break; } } \
      CFENCE(); pb_ = LOAD; \
    } } while (0)
#define UNDEF5(x) do { asm volatile("" : "=v"(x[0]), "=v"(x[1]), "=v"(x[2]), "=v"(x[3]), "=v"(x[4])); } while (0)
#define POLLM(LOADS, BAD) do { unsigned sp_ = 0; \
    do { CFENCE(); LOADS; if (__builtin_expect(__builtin_amdgcn_ballot_w64(BAD) == 0ull, 1)) break; __builtin_amdgcn_s_sleep(PSLEEP); } while (++sp_ <= SPIN_CAP); } while (0)

__global__ __launch_bounds__(256, 2) void fused_decode(const u16* xin, const u16* kc, const u16* vc, const u16* wq, const u16* wo,
                                                        const u16* w1, const u16* b1, const u16* w2, const u16* b2, u16* out,
                                                        unsigned char* ws) {
  extern __shared__ __attribute__((aligned(16))) unsigned char smem[];
  const unsigned LB = (unsigned)(uintptr_t)(LAS unsigned char*)smem;
  const unsigned tid = threadIdx.x, lane = tid & 63, g = lane >> 4, r = lane & 15;
  const unsigned w = __builtin_amdgcn_readfirstlane(tid >> 6);
  const unsigned bid = blockIdx.x, xcd = bid & 7, slot = bid >> 3, c = xcd * 32 + slot;
  const unsigned kvh = c >> 4, ii = c & 15;
  STAMP(0);
  const unsigned cb = ii >> 2, kq = ii & 3;
  const unsigned ab = ii >> 3, asl = ii & 7;
  const unsigned rb = ii & 3, cb3 = ii >> 2;
  const unsigned fi = xcd * 18 + slot, ri = (slot - 18) * 8 + xcd;
  const bool isF = slot < 18, isR = slot >= 18 && ri < NR;
  const unsigned rep = xcd + ((isF || isR) ? 0u : 8u);
  const unsigned pc = lane & 7;
  const unsigned hu = ((r >> 1) & 4) * 4 + 4 * g + ((r >> 1) & 3);

  const rsrc_t rs = __builtin_amdgcn_make_buffer_rsrc((void*)ws, 0, (int)(WS_USED), 0x00020000);
  if (w == 0) {
    glds(xin, lane * 16, LB + L_XG); glds(xin, 1024 + lane * 16, LB + L_XG + 1024);
    if (lane < 16) glds(xin, 2048 + lane * 16, LB + L_XG + 2048);
  }
  v2u ep = ald64c(rs, WS_E);
  const size_t kvbase = ((size_t)(ab * 16 + kvh) * SK + (SK - WIN) + asl * 128 + w * 32) * DH;
  {
    const unsigned rowl = lane >> 3, s16 = lane & 7;
    const unsigned fsw = ((lane >> 4) & 1) | ((w & 1) << 1);
    const unsigned chunk = ((((s16 >> 1) ^ fsw) << 1) | (s16 & 1));
    const unsigned voff = (144 * kq + 8 * w + rowl) * 8192 + (kvh * 256 + 64 * cb) * 2 + chunk * 16;
#pragma unroll
    for (int k = 0; k < 4; ++k) glds(wq, voff + k * 32 * 8192, LB + L_WQ + (w + 4 * k) * 1024);
    if (w < 2) glds(wq, voff + 4 * 32 * 8192, LB + L_WQ + (w + 16) * 1024);
  }
  {
    const unsigned sidx = (ab * 8 + asl) * 2 + (w >> 1);
    st32(rs, lane * 4, O0_OFF + kvh * O_G + sidx * 512 + (w & 1) * 256, SENT);
    st64(rs, ((w & 1) == 0 && lane == 0) ? 0u : OOB, ML0_OFF + kvh * ML_G + sidx * 64, v2u{SENT, SENT});
  }
  {
    const u16* Kb = kc + kvbase;
    const unsigned rowl = lane >> 4, vK = rowl * 256;
#pragma unroll
    for (int k = 0; k < 8; ++k) glds(Kb, vK + k * 1024 + ((r ^ ((4 * k + rowl) & 15)) * 16), LB + L_W1 + w * 8192 + k * 1024);
  }
  const u16* Vb = vc + kvbase;
  const unsigned vV0 = g * 256 + ((r ^ (g << 1)) * 16), vV1 = (4 + g) * 256 + ((r ^ ((4 + g) << 1)) * 16);
#pragma unroll
  for (int k = 0; k < VEARLY; ++k) { glds(Vb, vV0 + k * 2048, LB + L_V + w * 8192 + k * 2048); glds(Vb, vV1 + k * 2048, LB + L_V + w * 8192 + k * 2048 + 1024); }
  CFENCE();
  const unsigned q4 = r >> 2, p4 = r & 3;
  const unsigned aWQ = LB + L_WQ + (8 * g + q4) * 128 + ((w ^ (((q4 >> 1) & 1) | ((g & 1) << 1))) * 32) + 8 * p4;
  const unsigned aWQ4 = g < 2 ? aWQ + 4 * 4096 : aWQ;
  STAMP(1);
  VMWAITN(2 + 8 + 2 * VEARLY);
  asm volatile("" : "+v"(ep));
  if (tid < 8) *(LAS v4u*)(LB + L_ZERO + tid * 16) = v4u{0u, 0u, 0u, 0u};
  BAR();
  const unsigned tagl = ep.x + 1u, tagh = ep.y + (tagl == 0u ? 1u : 0u);
#pragma unroll
  for (int k = VEARLY; k < VEARLY + VMID; ++k) { glds(Vb, vV0 + k * 2048, LB + L_V + w * 8192 + k * 2048); glds(Vb, vV1 + k * 2048, LB + L_V + w * 8192 + k * 2048 + 1024); }
  STAMP(2);
  v4u qv, vf[8], kf[8];
  v4u hA[4] = {{0u, 0u, 0u, 0u}, {0u, 0u, 0u, 0u}, {0u, 0u, 0u, 0u}, {0u, 0u, 0u, 0u}};
  float cs = 0.f, cs1 = 0.f;

  {
    v4u wf[5], xf[5];
    const unsigned aX = LB + L_XG + ((r & 1) * D + 144 * kq + 8 * g) * 2;
#pragma unroll
    for (int s = 0; s < 4; ++s) xf[s] = *(LAS v4u*)(aX + 64 * s);
    xf[4] = *(LAS v4u*)(g < 2 ? aX + 256 : LB + L_ZERO);
#pragma unroll
    for (int s = 0; s < 4; ++s) wf[s] = trfrag(aWQ + s * 4096, aWQ + s * 4096 + 512);
    wf[4] = trfrag(aWQ4, aWQ4 + 512);
    if (r == 2) {
#pragma unroll
      for (int s = 0; s < 4; ++s) xf[s] = v4u{ONES, ONES, ONES, ONES};
      if (g < 2) xf[4] = v4u{ONES, ONES, ONES, ONES};
    }
    STAMP(3);
    f32x4 acc = {0.f, 0.f, 0.f, 0.f};
#pragma unroll
    for (int s = 0; s < 5; ++s) acc = mfma(wf[s], xf[s], acc);
    const unsigned vq0 = r * 128 + g * 32, sq0 = Q0_OFF + kvh * Q0_G + (kq * 16 + 4 * cb + w) * 512;
    VMWAITN(8 + 2 * (VEARLY + VMID));
    if (r < 3) {
      st128(rs, vq0, sq0, v4u{asu(acc[0]), tagl, asu(acc[1]), tagl});
      st128(rs, vq0 + 16, sq0, v4u{asu(acc[2]), tagl, asu(acc[3]), tagl});
    }
  }
  STAMP(4);
  if (w == 3) {
#pragma unroll
    for (int k = 0; k < 2; ++k) st128(rs, lane * 16 + k * 1024, WS_CB + c * CLR_PER_CU, v4u{SENT, SENT, SENT, SENT});
  }
#pragma unroll
  for (int k = VEARLY + VMID; k < 4; ++k) { glds(Vb, vV0 + k * 2048, LB + L_V + w * 8192 + k * 2048); glds(Vb, vV1 + k * 2048, LB + L_V + w * 8192 + k * 2048 + 1024); }
  STAMP(35);
  const rsrc_t rb2 = __builtin_amdgcn_make_buffer_rsrc((void*)b2, 0, D * 2, 0x00020000);
  const rsrc_t rb1 = __builtin_amdgcn_make_buffer_rsrc((void*)b1, 0, FF * 2, 0x00020000);
  v2u b2r = ald64(rb2, isR ? (16 * ri + 4 * (pc & 3)) * 2 : OOB);
  unsigned b1r = ald16(rb1, isF ? (32 * fi + hu) * 2 : OOB);
  unsigned tw0, tw1, tw2, tw3;
  tw0 = aldw(wo + (size_t)(kvh * 256 + 64 * rb) * D);
  tw1 = aldw(wo + (size_t)(kvh * 256 + 64 * rb + 63) * D);
  tw2 = aldw(w1 + (size_t)(144 * w) * FF + (isF ? 32 * fi : 0));
  tw3 = aldw(w2 + (size_t)(isF ? 32 * fi : 0) * D);
  Stats st;
  float xres[4], b2v[4], b1v;
  {
    v4u xs[3];
    {
      unsigned l2 = lane; OPAQUE(l2);
      const unsigned a2 = LB + L_XG + (((l2 >> 1) & 1) * D + 16 * (l2 >> 2) + 8 * (l2 & 1)) * 2;
      xs[0] = *(LAS v4u*)(a2); xs[1] = *(LAS v4u*)(a2 + 512); xs[2] = *(LAS v4u*)(l2 < 16 ? a2 + 1024 : LB + L_ZERO);
    }
    const v2u xr0 = *(LAS v2u*)(LB + L_XG + (isR ? ((pc >> 2) * D + 16 * ri + 4 * (pc & 3)) * 2 : 0));
    float sa = 0.f, sq = 0.f;
#pragma unroll
    for (int k = 0; k < 3; ++k) {
      sa = dot2(xs[k].x, ONES, sa); sa = dot2(xs[k].y, ONES, sa); sa = dot2(xs[k].z, ONES, sa); sa = dot2(xs[k].w, ONES, sa);
      sq = dot2(xs[k].x, xs[k].x, sq); sq = dot2(xs[k].y, xs[k].y, sq); sq = dot2(xs[k].z, xs[k].z, sq); sq = dot2(xs[k].w, xs[k].w, sq);
    }
    float t = t32_sum(sa, sq);
    { auto s = __builtin_amdgcn_permlane16_swap(asu(t), asu(t), false, false); unsigned a = s[0], b = s[1]; t = asf(a) + asf(b); }
    t += dpp<ROR8>(t); t += dpp<ROR4>(t); t += dpp<XOR1>(t);
    st = mkstats(rdl(t, ab * 2), rdl(t, 32 + ab * 2));
    xres[0] = bflo(xr0.x); xres[1] = bfhi(xr0.x); xres[2] = bflo(xr0.y); xres[3] = bfhi(xr0.y);
    PIN(st.m); PIN(st.rden); PIN(xres[0]); PIN(xres[1]); PIN(xres[2]); PIN(xres[3]);
  }
  STAMP(38);
  if (w == 3) {
    VMWAITN(2 * (4 - VEARLY - VMID) + 6);
    if (lane == 0) st64(rs, c * 128, WS_H0, v2u{tagl, tagh});
  }
  VMWAITN(VLEFT);
  STAMP(37);
  const unsigned vq = (g * 16 + 4 * w + q4) * 512 + p4 * 32, sq0r = Q0_OFF + kvh * Q0_G;
  unsigned sq0p = sq0r; PINS(sq0p);
  v4u qa = ald128c(rs, vq + ab * 128, sq0p), qb = ald128c(rs, vq + ab * 128 + 16, sq0p);
  v4u ca = ald128c(rs, vq + 256, sq0p), cb2 = ald128c(rs, vq + 272, sq0p);
  {
    const u16* Wb = wo + (size_t)(kvh * 256 + 64 * rb) * D + 144 * cb3;
#pragma unroll
    for (int k = 0; k < 5; ++k) {
      const unsigned piece = (k == 4 && w >= 2) ? w + 12 : w + 4 * k;
      const unsigned n = 64 * piece + lane;
      glds(Wb, 16 * n + 864 * ((n * 3641u) >> 16), LB + L_WO + piece * 1024);
    }
  }
  CFENCE();
  unsigned ln = lane; OPAQUE(ln);
  const unsigned g_ = ln >> 4, r_ = ln & 15, q4_ = r_ >> 2, p4_ = r_ & 3, pc_ = ln & 7;
  const unsigned hu_ = ((r_ >> 1) & 4) * 4 + 4 * g_ + ((r_ >> 1) & 3);
  const unsigned jm = ((g_ & 1) << 1) | (g_ >> 1);
  unsigned vXg = (ln >> 2) * 128 + (ln & 3) * 16;
  unsigned aXGw = LB + L_XG + w * 576 + ln * 16;
  unsigned aXGr = LB + L_XG + w * 576 + (g_ >> 1) * 64 + ((r_ & 1) * 2 + (g_ & 1)) * 16;
  unsigned aXGr4 = g_ < 2 ? aXGr + 512 : LB + L_ZERO;
  unsigned vXPw = g_ * (4 * XP_R) + r_ * 8, vFPw = g_ * FP_R + r_ * 8;
  unsigned vXs = ln < NR ? ln * 128 : OOB;
  unsigned vRec = (r_ & 1) * 64 + g_ * 16;
  unsigned vRed = (ln >> 3) * 128 + pc_ * 16;
  unsigned vRed4 = ln < 32 ? vRed + 4096 : OOB;
  unsigned vQ = (g_ * 16 + 4 * w + q4_) * 128 + ab * 64 + p4_ * 16;
  unsigned vO = (ln >> 3) * 1024 + pc_ * 16, vML = (ln >> 3) * 128;
  unsigned vOw = ln * 4;
  unsigned vXo = (ln >> 3) * X_REP + pc_ * 8;
  unsigned vSo = (ln >> 3) * X_REP + (pc_ >> 2) * 8;
  unsigned aQIw = LB + L_QI + (64 * w + 4 * r_ + jm) * 2, aQIr = LB + L_QI + ((r_ & 1) * 128 + 8 * g_) * 2;
  unsigned aV = LB + L_V + w * 8192 + (4 * g_ + q4_) * 256 + (4 * (g_ & 1) + q4_) * 32 + 8 * p4_;
  unsigned aOWw = LB + L_OW + (w * 128 + r_) * 8;
  unsigned aMw = LB + L_ML + w * 16 + (r_ & 1) * 4;
  unsigned aOWr = LB + L_OW + (64 * (w & 1) + ln) * 8 + (w >> 1) * 4;
  unsigned aMr = LB + L_ML + (w >> 1) * 4;
  unsigned aOIw = LB + L_OI + ((w >> 1) * 64 + 32 * (w & 1) + 8 * (pc_ & 3) + 4 * (pc_ >> 2) + jm) * 2, aOIr = LB + L_OI + ((r_ & 1) * 64 + 8 * g_) * 2;
  unsigned aOA0 = (r_ & 14) == 0 ? aOIr : LB + L_ZERO, aOA1 = (r_ & 14) == 4 ? aOIr : LB + L_ZERO, aOA2 = (r_ & 14) == 8 ? aOIr : LB + L_ZERO;
  unsigned aWO = LB + L_WO + (4 * g_ + q4_) * 288 + 32 * w + 8 * p4_;
  unsigned aWO8 = LB + L_WO + (4 * g_ + q4_) * 288 + 32 * 8 + 8 * p4_;
  unsigned aRSw = LB + L_RS + (w * 32 + (jm & 1) * 16 + 2 * pc_ + (jm >> 1)) * 4, aRSr = LB + L_RS + pc_ * 16;
  unsigned aW1 = LB + L_W1 + w * 9216 + (8 * g_ + q4_) * 64 + 8 * p4_ + (g_ & 1) * 32;
  unsigned aW14 = g_ < 2 ? aW1 + 4 * 2048 : aW1;
  unsigned aW2 = LB + L_W2 + w * 9216 + (4 * g_ + q4_) * 288 + 8 * p4_;
  unsigned aRDw = LB + L_RD + (w * 3 + r_) * 128 + g_ * 16;
  unsigned aST = LB + L_ST + (r_ & 1) * 8; PIN(aST);
  unsigned aRDr = LB + L_RD + (r_ & 1) * 128 + hu_ * 4;
  unsigned v18[9];
#pragma unroll
  for (int k = 0; k < 9; ++k) { const unsigned n = 64 * k + ln; v18[k] = 16 * n + 864 * ((n * 3641u) >> 16); PIN(v18[k]); }
  unsigned lt36 = ln < NR ? 0xFFFFFFFFu : 0u; PIN(lt36);
  PIN(vXg); PIN(aXGw); PIN(aXGr); PIN(aXGr4); PIN(vXPw); PIN(vFPw); PIN(aOA0); PIN(aOA1); PIN(aOA2); PIN(vXs); PIN(vRec); PIN(vRed); PIN(vRed4); PIN(vQ); PIN(vO); PIN(vML); PIN(vOw); PIN(vXo); PIN(vSo);
  PIN(aQIw); PIN(aQIr); PIN(aV); PIN(aOWw); PIN(aMw); PIN(aOWr); PIN(aMr); PIN(aOIw);
  PIN(aWO); PIN(aWO8); PIN(aRSw); PIN(aRSr); PIN(aW1); PIN(aW14); PIN(aW2); PIN(aRDw); PIN(aRDr);
  unsigned kvh_ = kvh, w_ = w; PINS(kvh_); PINS(w_);
  const unsigned sidx_w = (ab * 8 + asl) * 2 + (w_ >> 1);
  unsigned sOw = O0_OFF + kvh_ * O_G + sidx_w * 512 + (w_ & 1) * 256, sMLw = ML0_OFF + kvh_ * ML_G + sidx_w * 64;
  unsigned sOr = O0_OFF + kvh_ * O_G + ((w_ >> 1) * 16 + (rb >> 1)) * 512 + (rb & 1) * 256 + (w_ & 1) * 128;
  unsigned sMLr = ML0_OFF + kvh_ * ML_G + ((w_ >> 1) * 16 + (rb >> 1)) * 64;
  unsigned sXPw = XP0_OFF + 9 * cb3 * XP_R + (kvh_ * 4 + rb) * 128 + w_ * XP_R;
  unsigned sXPr = XP0_OFF + ri * XP_R + w_ * 2048;
  unsigned sX1w = X10_OFF + ri * 128, sX1r = X10_OFF + rep * X_REP + 9 * w_ * 128, sXS1r = XS10_OFF + rep * X_REP;
  unsigned sFPw = FP0_OFF + 9 * w_ * FP_R + fi * 128, sFPr = FP0_OFF + ri * FP_R + w_ * 36 * 128;
  unsigned sQ1w = Q1_OFF + kvh_ * Q_G + (kq * 16 + 4 * cb + w_) * 128, sQ1r = Q1_OFF + kvh_ * Q_G;
  unsigned sX2r = X2_OFF + rep * X_REP + 9 * kq * 128, sXS2r = XS2_OFF + rep * X_REP, sX2w = X2_OFF + ri * 128;
  PINS(sOw); PINS(sMLw); PINS(sOr); PINS(sMLr); PINS(sXPw); PINS(sXPr); PINS(sX1w); PINS(sX1r); PINS(sXS1r); PINS(sFPw); PINS(sFPr);
  PINS(sQ1w); PINS(sQ1r); PINS(sX2r); PINS(sXS2r); PINS(sX2w);
  asm volatile("s_waitcnt vmcnt(5)" : "+v"(qa), "+v"(qb), "+v"(ca), "+v"(cb2), "+v"(b2r), "+v"(b1r), "+v"(tw0), "+v"(tw1), "+v"(tw2), "+v"(tw3) :: "memory");
  {
#define Q0OK() __all(qa.y == tagl && qa.w == tagl && qb.y == tagl && qb.w == tagl && ca.y == tagl && ca.w == tagl && cb2.y == tagl && cb2.w == tagl)
    if (__builtin_expect(!Q0OK(), 0)) {
      for (unsigned sp = 0; sp <= SPIN_CAP; ++sp) {
        CFENCE();
        qa = ld128(rs, vq + ab * 128, sq0r); qb = ld128(rs, vq + ab * 128 + 16, sq0r);
        ca = ld128(rs, vq + 256, sq0r); cb2 = ld128(rs, vq + 272, sq0r);
        if (Q0OK()) break;
        __builtin_amdgcn_s_sleep(2);
      }
    }
#undef Q0OK
    b2v[0] = bflo(b2r.x); b2v[1] = bfhi(b2r.x); b2v[2] = bflo(b2r.y); b2v[3] = bfhi(b2r.y);
    b1v = bflo(b1r);
    STAMP(5);
#pragma unroll
    for (int j = 0; j < 8; ++j) kf[j] = *(LAS v4u*)(LB + L_W1 + w * 8192 + ((j >> 2) * 16 + r) * 256 + ((((j & 3) * 4 + g) ^ r) * 16));
    {
      unsigned a = aV; OPAQUE(a);
#pragma unroll
      for (int dt = 0; dt < 8; ++dt) vf[dt] = trfrag(a ^ (dt * 32), (a ^ (dt * 32)) + 16 * 256);
    }
    qv = v4u{qa.x, qa.z, qb.x, qb.z};
    cs = trsum4(f32x4{asf(ca.x), asf(ca.z), asf(cb2.x), asf(cb2.z)});
  }

#pragma unroll 1
  for (int lay = 0; lay < 2; ++lay) {
    const unsigned dO = lay ? O1_OFF - O0_OFF : 0u, dML = lay ? ML1_OFF - ML0_OFF : 0u, dXP = lay ? XP1_OFF - XP0_OFF : 0u;
    const unsigned dX1 = lay ? X11_OFF - X10_OFF : 0u, dFP = lay ? FP_L : 0u;
    {
      STAMP(6 + lay * 16);
      const float q = trsum4(asf4(qv));
      const unsigned qb = pk((q - st.m * cs) * st.rden, 0.f);
      *(LAS u16*)(aQIw) = (u16)qb;
    }
    BAR();
    {
      v4u qf[4];
#pragma unroll
      for (int s = 0; s < 4; ++s) qf[s] = *(LAS v4u*)(aQIr + 64 * s);
      f32x4 s0 = {0.f, 0.f, 0.f, 0.f}, s1 = {0.f, 0.f, 0.f, 0.f};
#pragma unroll
      for (int s = 0; s < 4; ++s) { s0 = mfma(kf[s], qf[s], s0); s1 = mfma(kf[4 + s], qf[s], s1); }
      float sc[8];
#pragma unroll
      for (int j = 0; j < 4; ++j) { sc[j] = s0[j]; sc[4 + j] = s1[j]; }
      constexpr float EK = LOG2E * 0.08838834764831845f;
      float mx = fmx3(fmx3(sc[0], sc[1], sc[2]), fmx3(sc[3], sc[4], sc[5]), fmx(sc[6], sc[7]));
      mx = x16_max(mx); mx = x32_max(mx);
      const float mb = -mx * EK;
      float e[8];
#pragma unroll
      for (int j = 0; j < 8; ++j) e[j] = ex2(__builtin_fmaf(sc[j], EK, mb));
      v4u pb; pb.x = pk(e[0], e[1]); pb.y = pk(e[2], e[3]); pb.z = pk(e[4], e[5]); pb.w = pk(e[6], e[7]);
      f32x4 oa[8];
#pragma unroll
      for (int dt = 0; dt < 8; ++dt) oa[dt] = mfma(pb, vf[dt], f32x4{0.f, 0.f, 0.f, 0.f});
      const f32x4 la = mfma(pb, v4u{ONES, ONES, ONES, ONES}, f32x4{0.f, 0.f, 0.f, 0.f});
#pragma unroll
      for (int dt = 0; dt < 8; ++dt) KEEP4(oa[dt]);
      KEEP4(la);
      if (g == 0) {
#pragma unroll
        for (int dt = 0; dt < 8; ++dt) { v2u t; t.x = asu(oa[dt][0]); t.y = asu(oa[dt][1]); *(LAS v2u*)(aOWw + dt * 128) = t; }
        if (r < 2) { *(LAS float*)(aMw) = mb; *(LAS float*)(aMw + 8) = r ? la[1] : la[0]; }
      }
    }
    BAR();
    {
      float m[4], l[4], o[4];
#pragma unroll
      for (int k = 0; k < 4; ++k) {
        m[k] = *(LAS float*)(aMr + k * 16);
        l[k] = *(LAS float*)(aMr + k * 16 + 8);
        o[k] = *(LAS float*)(aOWr + k * 1024);
      }
      const float M = __builtin_elementwise_minimum(__builtin_elementwise_minimum(m[0], m[1]), __builtin_elementwise_minimum(m[2], m[3]));
      float f[4];
#pragma unroll
      for (int k = 0; k < 4; ++k) f[k] = ex2(M - m[k]);
      const float os = __builtin_fmaf(f[0], o[0], f[1] * o[1]) + __builtin_fmaf(f[2], o[2], f[3] * o[3]);
      const float lsum = __builtin_fmaf(f[0], l[0], f[1] * l[1]) + __builtin_fmaf(f[2], l[2], f[3] * l[3]);
      st32(rs, vOw, (sOw + dO), asu(os));
      if ((w & 1) == 0 && lane == 0) st64(rs, 0, (sMLw + dML), v2u{asu(M), asu(lsum)});
    }
    STAMP(7 + lay * 16);
    if (lay == 0 && isF) {
      unsigned wo_ = w, fo_ = fi; PINS(wo_); PINS(fo_);
      const u16* W2b = w2 + (size_t)(32 * fo_) * D + 144 * wo_;
#pragma unroll
      for (int k = 0; k < W2EARLY; ++k) glds(W2b, v18[k], LB + L_W2 + wo_ * 9216 + k * 1024);
    }
    if (lay == 0) {
      const v4u ones = {SENT, SENT, SENT, SENT};
      unsigned a0 = sQ1w, a1 = sOw, a2 = sMLw, a3 = sXPw, a4 = sFPw, a5 = sX1w;
      PINS(a0); PINS(a1); PINS(a2); PINS(a3); PINS(a4); PINS(a5);
      st32(rs, vOw, a1 + (O1_OFF - O0_OFF), SENT);
      if ((w & 1) == 0 && lane == 0) st64(rs, 0, a2 + (ML1_OFF - ML0_OFF), v2u{SENT, SENT});
      const v2u ones2 = {SENT, SENT};
      if (r < 2) st128(rs, vRec, a0, ones);
      if (g < (w == 0 ? 3u : 2u)) st64(rs, vXPw, a3 + (XP1_OFF - XP0_OFF), ones2);
      if (isF) {
        st64(rs, vFPw, a4, ones2); st64(rs, vFPw, a4 + 4 * FP_R, ones2);
        st64(rs, vFPw, a4 + FP_L, ones2); st64(rs, vFPw, a4 + FP_L + 4 * FP_R, ones2);
        if (g == 0) { st64(rs, vFPw, a4 + 8 * FP_R, ones2); st64(rs, vFPw, a4 + FP_L + 8 * FP_R, ones2); }
      }
      if (isR && w == 0) {
        st64(rs, vXo, a5 + (X11_OFF - X10_OFF), v2u{SENT, SENT});
        if ((pc & 3) == 0) st64(rs, vSo, a5 + (XS11_OFF - X10_OFF), v2u{SENT, SENT});
      }
    }
    {
      v4u ov; v2u ml;
      delay<DLY_H2>();
      if (lay == 0) {
        for (unsigned sp = 0; sp <= SPIN_CAP; ++sp) {
          CFENCE();
          const v2u t0 = ld64(rs, tid * 128, WS_H0);
          ov = ld128(rs, vO, sOr); ml = ld64(rs, vML, sMLr);
          if (__all(t0.x == tagl && t0.y == tagh && um3(vmax1(ov), ml.x, ml.y) != SENT)) break;
          __builtin_amdgcn_s_sleep(PSLEEP);
        }
      } else {
        POLL(ov = ld128(rs, vO, (sOr + dO)); ml = ld64(rs, vML, (sMLr + dML)), um3(vmax1(ov), ml.x, ml.y));
      }
      STAMP(8 + lay * 16);
      const float ms = asf(ml.x);
      float M = __builtin_elementwise_minimum(ms, dpp<ROR8>(ms));
      { auto s = __builtin_amdgcn_permlane16_swap(asu(M), asu(M), false, false); unsigned x = s[0], y = s[1]; M = __builtin_elementwise_minimum(asf(x), asf(y)); }
      { auto s = __builtin_amdgcn_permlane32_swap(asu(M), asu(M), false, false); unsigned x = s[0], y = s[1]; M = __builtin_elementwise_minimum(asf(x), asf(y)); }
      const float f = ex2(M - ms);
      float lsum = f * asf(ml.y);
      f32x4 o = asf4(ov) * f;
      lsum += dpp<ROR8>(lsum);
      float t = trsum4(o);
      { auto s = __builtin_amdgcn_permlane16_swap(asu(lsum), asu(lsum), false, false); unsigned x = s[0], y = s[1]; lsum = asf(x) + asf(y); }
      t += dpp<ROR8>(t);
      { auto s = __builtin_amdgcn_permlane32_swap(asu(lsum), asu(lsum), false, false); unsigned x = s[0], y = s[1]; lsum = asf(x) + asf(y); }
      const unsigned ob = pk(t * rcp(lsum), 0.f);
      *(LAS u16*)(aOIw) = (u16)ob;
      if (lay == 0) VMWAIT(0);
      BAR();
      const v4u oa00 = *(LAS v4u*)(aOA0), oa01 = *(LAS v4u*)(aOA0 + 64), oa10 = *(LAS v4u*)(aOA1), oa11 = *(LAS v4u*)(aOA1 + 64);
      const v4u wf0 = trfrag(aWO, aWO + 16 * 288), wf1 = trfrag(aWO + 32 * 288, aWO + 48 * 288);
      const v4u wf2 = trfrag(aWO + 128, aWO + 128 + 16 * 288), wf3 = trfrag(aWO + 128 + 32 * 288, aWO + 128 + 48 * 288);
      f32x4 acc = mfma(oa00, wf0, f32x4{0.f, 0.f, 0.f, 0.f}), acc2 = mfma(oa10, wf2, f32x4{0.f, 0.f, 0.f, 0.f});
      if (w == 0) {
        const v4u oa20 = *(LAS v4u*)(aOA2), oa21 = *(LAS v4u*)(aOA2 + 64);
        const v4u wf4 = trfrag(aWO8, aWO8 + 16 * 288), wf5 = trfrag(aWO8 + 32 * 288, aWO8 + 48 * 288);
        acc = mfma(oa20, wf4, acc); acc2 = mfma(oa21, wf5, acc2);
      }
      acc = mfma(oa01, wf1, acc); acc2 = mfma(oa11, wf3, acc2);
      if (g < (w == 0 ? 3u : 2u)) st64(rs, vXPw, (sXPw + dXP), v2u{asu(acc[0] + acc2[0]), asu(acc[1] + acc2[1])});
    }
    STAMP(9 + lay * 16);
    if (lay == 0 && isF) {
      unsigned ln2 = lane, wo_ = w, fo_ = fi; OPAQUE(ln2); PINS(wo_); PINS(fo_);
      const unsigned s4 = ln2 & 3, chunk = (((s4 >> 1) ^ ((ln2 >> 5) & 1)) << 1) | (s4 & 1);
      const unsigned v1 = (144 * wo_ + (ln2 >> 2)) * (FF * 2) + 64 * fo_ + chunk * 16;
#pragma unroll
      for (int k = W1EARLY; k < 9; ++k) glds(w1, v1 + k * 16 * (FF * 2), LB + L_W1 + wo_ * 9216 + k * 1024);
      const u16* W2b = w2 + (size_t)(32 * fo_) * D + 144 * wo_;
#pragma unroll
      for (int k = W2EARLY; k < 9; ++k) glds(W2b, v18[k], LB + L_W2 + wo_ * 9216 + k * 1024);
    }
    if (isR) {
      v4u pv0, pv1;
      delay<DLY_H3>();
      POLL(pv0 = ld128(rs, vRed, (sXPr + dXP)); pv1 = ld128(rs, vRed + 1024, (sXPr + dXP)), vmax2(pv0, pv1));
      STAMP(10 + lay * 16);
      float t = trsum4(asf4(pv0) + asf4(pv1));
      t += dpp<ROR8>(t);
      *(LAS float*)(aRSw) = t;
      BAR();
      if (w == 0) {
        const f32x4 s0 = *(LAS f32x4*)(aRSr), s1 = *(LAS f32x4*)(aRSr + 128), s2 = *(LAS f32x4*)(aRSr + 256), s3 = *(LAS f32x4*)(aRSr + 384);
        const f32x4 s = (s0 + s1) + (s2 + s3);
        const unsigned y0 = pko(s[0], s[1]), y1 = pko(s[2], s[3]);
        v2u xo; xo.x = san(pko(xres[0] + bflo(y0), xres[1] + bfhi(y0))); xo.y = san(pko(xres[2] + bflo(y1), xres[3] + bfhi(y1)));
        xres[0] = bflo(xo.x); xres[1] = bfhi(xo.x); xres[2] = bflo(xo.y); xres[3] = bfhi(xo.y);
        float sa = (xres[0] + xres[1]) + (xres[2] + xres[3]);
        float sq = __builtin_fmaf(xres[0], xres[0], xres[1] * xres[1]) + __builtin_fmaf(xres[2], xres[2], xres[3] * xres[3]);
        sa += dpp<XOR1>(sa); sq += dpp<XOR1>(sq); sa += dpp<XOR2>(sa); sq += dpp<XOR2>(sq);
        if ((pc & 3) == 0) st64(rs, vSo, (sX1w + dX1) + X1_L, v2u{asu(sa), asu(sq)});
        CFENCE();
        st64(rs, vXo, (sX1w + dX1), xo);
      }
      STAMP(11 + lay * 16);
    }
    if (isF) {
      v4u a0[5], a1[5];
      delay<DLY_H4>();
      if (lay == 0) VMWAIT(0);
#pragma unroll
      for (int s = 0; s < 4; ++s) {
        a0[s] = trfrag(aW1 + s * 2048, aW1 + s * 2048 + 256);
        a1[s] = trfrag((aW1 ^ 32) + s * 2048, (aW1 ^ 32) + s * 2048 + 256);
      }
      a0[4] = trfrag(aW14, aW14 + 256); a1[4] = trfrag(aW14 ^ 32, (aW14 ^ 32) + 256);
      v4u x1[5], s1v, xg0;
      asm volatile("" : "=v"(xg0));
      if (w == 3) {
        POLLM(
          s1v = ld128(rs, vXs, (sXS1r + dX1));
          if (lane < NR) xg0 = ld128(rs, vXg, (sX1r + dX1)),
          um2(vmax1(xg0) & lt36, vmax1(s1v)) == SENT);
      } else {
        POLLM(if (lane < NR) xg0 = ld128(rs, vXg, (sX1r + dX1)), (vmax1(xg0) & lt36) == SENT);
      }
      if (lane < NR) *(LAS v4u*)(aXGw) = xg0;
      asm volatile("s_waitcnt lgkmcnt(0)" ::: "memory");
#pragma unroll
      for (int s = 0; s < 4; ++s) x1[s] = *(LAS v4u*)(aXGr + s * 128);
      x1[4] = *(LAS v4u*)(aXGr4);
      if (lay == 0 && r == 2) {
#pragma unroll
        for (int s = 0; s < 4; ++s) x1[s] = v4u{ONES, ONES, ONES, ONES};
        if (g < 2) x1[4] = v4u{ONES, ONES, ONES, ONES};
      }
      STAMP(12 + lay * 16);
      f32x4 h0 = {0.f, 0.f, 0.f, 0.f}, h1 = {0.f, 0.f, 0.f, 0.f};
#pragma unroll
      for (int s = 0; s < 5; ++s) { h0 = mfma(a0[s], x1[s], h0); h1 = mfma(a1[s], x1[s], h1); }
      if (w == 3) {
        const float t = stats_reduce(s1v);
        auto s = __builtin_amdgcn_permlane32_swap(asu(t), asu(t), false, false);
        const unsigned x = s[0], y = s[1];
        const Stats z = mkstats(asf(x), asf(y));
        if (r == 0 && g < 2) *(LAS v2u*)(LB + L_ST + g * 8) = v2u{asu(z.m), asu(z.rden)};
      }
      if (r < 3) { *(LAS f32x4*)(aRDw) = h0; *(LAS f32x4*)(aRDw + 64) = h1; }
      BAR();
      const v2u smr = *(LAS v2u*)(aST);
      const float sm = asf(smr.x), sr = asf(smr.y);
      const float p0 = *(LAS float*)(aRDr), p1 = *(LAS float*)(aRDr + 384), p2 = *(LAS float*)(aRDr + 768), p3 = *(LAS float*)(aRDr + 1152);
      v4u w2f[9];
#pragma unroll
      for (int t = 0; t < 9; ++t) w2f[t] = trfrag(aW2 + 32 * t, aW2 + 32 * t + 16 * 288);
      const float hs = (p0 + p1) + (p2 + p3);
      if (lay == 0) {
        unsigned ac = aRDr; OPAQUE(ac); ac += (2 - (r & 1)) * 128;
        cs1 = (*(LAS float*)(ac) + *(LAS float*)(ac + 384)) + (*(LAS float*)(ac + 768) + *(LAS float*)(ac + 1152));
      }
      const float pre = rbf((hs - sm * cs1) * sr);
      const float a = rbf(pre + b1v);
      const float u = a * __builtin_fmaf(a * a, -2.0f * LOG2E * 0.7978845608028654f * 0.044715f, -2.0f * LOG2E * 0.7978845608028654f);
      const float hv = a * rcp(1.0f + ex2(u));
      const unsigned hp = pk(hv, dpp<SHL2>(hv));
#define HAV(G, C, CTRL) hA[G].C = (unsigned)__builtin_amdgcn_update_dpp((int)hA[G].C, (int)hp, CTRL, 0xf, 1 << G, true)
      HAV(0, x, 0xE4); HAV(0, y, 0x104); HAV(0, z, 0x108); HAV(0, w, 0x10C);
      HAV(1, x, 0x114); HAV(1, y, 0xE4); HAV(1, z, 0x104); HAV(1, w, 0x108);
      HAV(2, x, 0x118); HAV(2, y, 0x114); HAV(2, z, 0xE4); HAV(2, w, 0x104);
      HAV(3, x, 0x11C); HAV(3, y, 0x118); HAV(3, z, 0x114); HAV(3, w, 0xE4);
#undef HAV
      f32x4 f0 = mfma(hA[0], w2f[0], f32x4{0.f, 0.f, 0.f, 0.f}), f1 = mfma(hA[0], w2f[4], f32x4{0.f, 0.f, 0.f, 0.f});
      const f32x4 f2 = mfma(hA[0], w2f[8], f32x4{0.f, 0.f, 0.f, 0.f});
#pragma unroll
      for (int t = 1; t < 4; ++t) { f0 = mfma(hA[t], w2f[t], f0); f1 = mfma(hA[t], w2f[4 + t], f1); }
      st64(rs, vFPw, (sFPw + dFP), v2u{asu(f0[0]), asu(f0[1])});
      st64(rs, vFPw, (sFPw + dFP) + 4 * FP_R, v2u{asu(f1[0]), asu(f1[1])});
      if (g == 0) st64(rs, vFPw, (sFPw + dFP) + 8 * FP_R, v2u{asu(f2[0]), asu(f2[1])});
      STAMP(13 + lay * 16);
    }
    if (isR) {
      v4u pv[5];
      delay<DLY_H5>();
      POLL(
        for (int k = 0; k < 4; ++k) pv[k] = ld128(rs, vRed + k * 1024, (sFPr + dFP));
        pv[4] = ld128(rs, vRed4, (sFPr + dFP)),
        vmax5(pv));
      STAMP(14 + lay * 16);
      float t = trsum4(((asf4(pv[0]) + asf4(pv[1])) + (asf4(pv[2]) + asf4(pv[3]))) + asf4(pv[4]));
      t += dpp<ROR8>(t);
      *(LAS float*)(aRSw) = t;
      BAR();
      if (w == 0) {
        const f32x4 s0 = *(LAS f32x4*)(aRSr), s1 = *(LAS f32x4*)(aRSr + 128), s2 = *(LAS f32x4*)(aRSr + 256), s3 = *(LAS f32x4*)(aRSr + 384);
        const f32x4 s = (s0 + s1) + (s2 + s3);
        const unsigned y0 = pko(s[0], s[1]), y1 = pko(s[2], s[3]);
        const unsigned z0 = pko(bflo(y0) + b2v[0], bfhi(y0) + b2v[1]), z1 = pko(bflo(y1) + b2v[2], bfhi(y1) + b2v[3]);
        v2u xo; xo.x = pko(xres[0] + bflo(z0), xres[1] + bfhi(z0)); xo.y = pko(xres[2] + bflo(z1), xres[3] + bfhi(z1));
        if (lay == 0) {
          xo.x = san(xo.x); xo.y = san(xo.y);
          st64(rs, vXo, sX2w, xo);
          st64(rs, vXo, sX2w + 8 * X_REP, xo);
          xres[0] = bflo(xo.x); xres[1] = bfhi(xo.x); xres[2] = bflo(xo.y); xres[3] = bfhi(xo.y);
          float sa = (xres[0] + xres[1]) + (xres[2] + xres[3]);
          float sq = __builtin_fmaf(xres[0], xres[0], xres[1] * xres[1]) + __builtin_fmaf(xres[2], xres[2], xres[3] * xres[3]);
          sa += dpp<XOR1>(sa); sq += dpp<XOR1>(sq); sa += dpp<XOR2>(sa); sq += dpp<XOR2>(sq);
          if ((pc & 3) == 0) {
            st64(rs, vSo, sX2w + 16 * X_REP, v2u{asu(sa), asu(sq)});
            st64(rs, vSo, sX2w + 24 * X_REP, v2u{asu(sa), asu(sq)});
          }
        } else {
          unsigned ln2 = lane, ro_ = ri; OPAQUE(ln2); PINS(ro_);
          if (ln2 < 8) *(GAS v2u*)(out + (ln2 >> 2) * D + 16 * ro_ + 4 * (ln2 & 3)) = xo;
        }
      }
      STAMP(15 + lay * 16);
    }
    if (lay == 0) {
      v4u wf[5], xg[5], sv;
#pragma unroll
      for (int s = 0; s < 4; ++s) wf[s] = trfrag(aWQ + s * 4096, aWQ + s * 4096 + 512);
      wf[4] = trfrag(aWQ4, aWQ4 + 512);
      if (isR) delay<DLY_H6R>(); else if (isF) delay<DLY_H6F>(); else delay<DLY_H6I>();
      v4u xg0;
      asm volatile("" : "=v"(xg0));
      POLLM(if (lane < NR) xg0 = ld128(rs, vXg, sX2r), (vmax1(xg0) & lt36) == SENT);
      if (lane < NR) *(LAS v4u*)(aXGw) = xg0;
      sv = ld128(rs, vXs, sXS2r);
      asm volatile("s_waitcnt lgkmcnt(0)" ::: "memory");
#pragma unroll
      for (int s = 0; s < 4; ++s) xg[s] = *(LAS v4u*)(aXGr + s * 128);
      xg[4] = *(LAS v4u*)(aXGr4);
      STAMP(19);
      f32x4 acc = {0.f, 0.f, 0.f, 0.f};
#pragma unroll
      for (int s = 0; s < 5; ++s) acc = mfma(wf[s], xg[s], acc);
      if (r < 2) st128(rs, vRec, sQ1w, asu4(acc));
      STAMP(20);
      {
        unsigned a = aV; OPAQUE(a);
#pragma unroll
        for (int dt = 0; dt < 8; ++dt) vf[dt] = trfrag(a ^ (dt * 32), (a ^ (dt * 32)) + 16 * 256);
      }
      { unsigned sp_ = 0; while (__builtin_expect(__builtin_amdgcn_ballot_w64(vmax1(sv) == SENT) != 0ull, 0) && ++sp_ <= SPIN_CAP) { CFENCE(); sv = ld128(rs, vXs, sXS2r); } }
      const float t = stats_reduce(sv);
      st = mkstats(rdl(t, 16 * ab), rdl(t, 32 + 16 * ab));
      PIN(st.m); PIN(st.rden);
      STAMP(21);
      delay<DLY_H1>();
      POLL(qv = ld128(rs, vQ, sQ1r), vmax1(qv));
    }
  }
  if (c == 0 && tid == 0) st64(rs, 0, WS_E, v2u{tagl, tagh});
}

}

extern "C" void kernel_launch(void* const* d_in, const int* in_sizes, int n_in, void* d_out, int out_size, void* d_ws, size_t ws_size,
                              hipStream_t stream) {
  (void)in_sizes; (void)n_in; (void)out_size; (void)ws_size;
  (void)hipFuncSetAttribute((const void*)fk::fused_decode, hipFuncAttributeMaxDynamicSharedMemorySize, (int)fk::L_END);
  fk::fused_decode<<<256, 256, fk::L_END, stream>>>((const u16*)d_in[0], (const u16*)d_in[1], (const u16*)d_in[2], (const u16*)d_in[3],
                                                    (const u16*)d_in[4], (const u16*)d_in[6], (const u16*)d_in[7], (const u16*)d_in[8],
                                                    (const u16*)d_in[9], (u16*)d_out, (unsigned char*)d_ws);
}
```
